```python
import torch

def _softmax(x, axis=-1):
    return torch.softmax(x, dim=axis)
def _gelu(x):
    return torch.nn.functional.gelu(x, approximate="tanh")
def _silu(x):
    return torch.nn.functional.silu(x)
def _sigmoid(x):
    return torch.sigmoid(x)
def _layernorm(x, axis=-1, eps=1e-5):
    m = x.mean(dim=axis, keepdim=True)
    v = x.var(dim=axis, keepdim=True, unbiased=False)
    return (x - m) / torch.sqrt(v + eps)
def _scatter_add(tgt, idx, src, axis=0):
    return tgt.clone().index_add_(axis, idx, src)
def _segment_sum(src, seg, n):
    out = torch.zeros((n,) + src.shape[1:], dtype=src.dtype)
    return out.index_add_(0, seg, src)
def _pad(x, pw):
    flat = tuple(v for pair in reversed(pw) for v in pair)
    return torch.nn.functional.pad(x, flat)

kernel_name = 'interleave_layers__ssm_conv_s4__sliding_block__prompt'

def setup_inputs():

    _w = (lambda a: (lambda t: t.to(torch.bfloat16) if t.is_floating_point() else t)(torch.as_tensor(a)))
    def _A_setup():
        B, L, D, N = 1, 2048, 512, 128
        A_re = -(lambda _x: torch.exp(torch.as_tensor(_x)))((lambda *s: torch.randn(*s, dtype=torch.bfloat16) * 1.6)(D, N))
        A_im = torch.pi * (lambda *s: torch.randn(*s, dtype=torch.bfloat16) * 0.05)(D, N)
        B_re = (lambda *s: torch.randn(*s, dtype=torch.bfloat16) * 0.05)(D, N)
        B_im = (lambda *s: torch.randn(*s, dtype=torch.bfloat16) * 0.05)(D, N)
        C_re = (lambda *s: torch.randn(*s, dtype=torch.bfloat16) * 0.05)(D, N)
        C_im = (lambda *s: torch.randn(*s, dtype=torch.bfloat16) * 0.05)(D, N)
        log_dt = (lambda *s: torch.randn(*s, dtype=torch.bfloat16) * 0.05)(D) * 0.5 - 4.0
        Dskip = (lambda *s: torch.randn(*s, dtype=torch.bfloat16) * 0.05)(D)
        return {
            "u": _w((lambda *s: torch.randn(*s, dtype=torch.bfloat16) * 0.05)(B, L, D)),
            "A_re": _w(A_re),
            "A_im": _w(A_im),
            "B_re": _w(B_re),
            "B_im": _w(B_im),
            "C_re": _w(C_re),
            "C_im": _w(C_im),
            "log_dt": _w(log_dt),
            "Dskip": _w(Dskip),
        }
    _A_in = _A_setup()

    def _B_setup():
        B, S, D = 1, 2048, 512
        return {
            "x": _w((lambda *s: torch.randn(*s, dtype=torch.bfloat16) * 0.05)(B, S, D)),
            "wqkv": _w((lambda *s: torch.randn(*s, dtype=torch.bfloat16) * 0.05)(D, 3 * D)),
            "wo": _w((lambda *s: torch.randn(*s, dtype=torch.bfloat16) * 0.05)(D, D)),
        }
    _B_in = _B_setup()

    return {
        "A_u": _A_in["u"],
        "A_A_re": _A_in["A_re"],
        "A_A_im": _A_in["A_im"],
        "A_B_re": _A_in["B_re"],
        "A_B_im": _A_in["B_im"],
        "A_C_re": _A_in["C_re"],
        "A_C_im": _A_in["C_im"],
        "A_log_dt": _A_in["log_dt"],
        "A_Dskip": _A_in["Dskip"],
        "B_x": _B_in["x"],
        "B_wqkv": _B_in["wqkv"],
        "B_wo": _B_in["wo"],
    }

def reference(A_u, A_A_re, A_A_im, A_B_re, A_B_im, A_C_re, A_C_im, A_log_dt, A_Dskip, B_x, B_wqkv, B_wo):
    def _A_ref(u, A_re, A_im, B_re, B_im, C_re, C_im, log_dt, Dskip):
        Bsz, L, D = u.shape
        dt = torch.exp(log_dt)[:, None]
        A = A_re + 1j * A_im
        Bc = B_re + 1j * B_im
        Cc = C_re + 1j * C_im
        logdA = dt * A
        dB = dt * Bc
        l = (lambda x, dt: x.to(dt))(torch.arange(L), A_re.dtype)
        P = torch.exp(logdA[:, :, None] * l[None, None, :])
        K = torch.einsum("dn,dnl,dn->dl", Cc, P, dB).real
        L2 = 2 * L
        Kp = _pad(K, ((0, 0), (0, L)))
        up = (lambda x, *d: x.permute(*d))(u, 0, 2, 1)
        up = _pad(up, ((0, 0), (0, 0), (0, L)))
        Kf = (lambda x, axes=(-2,-1): torch.fft.rfft2(x.float(), dim=axes))(Kp[:, None, :], axes=(-2, -1))
        Uf = (lambda x, axes=(-2,-1): torch.fft.rfft2(x.float(), dim=axes))(up[:, :, None, :], axes=(-2, -1))
        Yf = Uf * Kf[None, :, :, :]
        y = (lambda x, s, axes=(-2,-1): torch.fft.irfft2(x, s=s, dim=axes).to(torch.bfloat16))(Yf, (1, L2), axes=(-2, -1))[:, :, 0, :L]
        y = (lambda x, *d: x.permute(*d))(y, 0, 2, 1) + u * Dskip[None, None, :]
        return y

    def _B_ref(x, wqkv, wo):
        B, S, D = x.shape
        H = 32; Dh = D // H; W = 256
        qkv = (lambda x, *d: x.permute(*d))((x @ wqkv).reshape(B, S, 3, H, Dh), 2, 0, 3, 1, 4)
        q, k, v = qkv[0], qkv[1], qkv[2]
        scores = torch.einsum("bhqd,bhkd->bhqk", q, k) * (Dh ** -0.5)
        idx = torch.arange(S)
        mask = (idx[:, None] - idx[None, :] >= 0) & (idx[:, None] - idx[None, :] < W)
        scores = torch.where(mask[None, None], scores, -1e9)
        p = _softmax(scores, axis=-1)
        o = torch.einsum("bhqk,bhkd->bhqd", p, v)
        return (lambda x, *d: x.permute(*d))(o, 0, 2, 1, 3).reshape(B, S, D) @ wo

    x = A_u
    x = x + _A_ref(_layernorm(x, axis=-1), A_A_re, A_A_im, A_B_re, A_B_im, A_C_re, A_C_im, A_log_dt, A_Dskip)
    x = x + _B_ref(_layernorm(x, axis=-1), B_wqkv, B_wo)
    x = x + _A_ref(_layernorm(x, axis=-1), A_A_re, A_A_im, A_B_re, A_B_im, A_C_re, A_C_im, A_log_dt, A_Dskip)
    x = x + _B_ref(_layernorm(x, axis=-1), B_wqkv, B_wo)
    return x
```

```cpp
#include <hip/hip_runtime.h>
#include <hip/hip_bf16.h>
#include <stdint.h>

typedef short bf16x8 __attribute__((ext_vector_type(8)));
typedef short bf16x4 __attribute__((ext_vector_type(4)));
typedef float f32x4 __attribute__((ext_vector_type(4)));
typedef float f32x16 __attribute__((ext_vector_type(16)));
typedef unsigned u32x4 __attribute__((ext_vector_type(4)));
typedef unsigned u32x2 __attribute__((ext_vector_type(2)));
typedef __bf16 bf2_t __attribute__((ext_vector_type(2)));
typedef float f2_t __attribute__((ext_vector_type(2)));
typedef unsigned short u16;
typedef __attribute__((address_space(3))) char lds_char;

#define DEV __device__ __forceinline__

constexpr int T = 2048, D = 512, NM = 128, H = 32;
constexpr int RROW = 2080;

constexpr size_t MB = 1024 * 1024;
constexpr size_t OFF_XT0 = 0, OFF_XT1 = 2 * MB, OFF_XT2 = 4 * MB, OFF_XT3 = 6 * MB;
constexpr size_t OFF_R = 8 * MB;
constexpr size_t OFF_WF = 11 * MB;
constexpr size_t OFF_WOF = 12 * MB + 512 * 1024;
constexpr size_t OFF_CS = 13 * MB;
constexpr size_t OFF_ST0 = 13 * MB + 64 * 1024;
constexpr size_t OFF_ACC = 13 * MB + 128 * 1024;
constexpr size_t OFF_QF = 14 * MB, OFF_KF = 16 * MB, OFF_VF = 18 * MB, OFF_OF = 20 * MB;

DEV float bf2f(unsigned v) { return __uint_as_float(v << 16); }
DEV float bflo(unsigned v) { return __uint_as_float(v << 16); }
DEV float bfhi(unsigned v) { return __uint_as_float(v & 0xffff0000u); }
DEV unsigned pk(float lo, float hi) {
    f2_t v = {lo, hi};
    return __builtin_bit_cast(unsigned, __builtin_convertvector(v, bf2_t));
}
DEV float rbf(float x) { return __uint_as_float(pk(x, 0.f) << 16); }

#define MFMA16(a, b, c) __builtin_amdgcn_mfma_f32_16x16x32_bf16(a, b, c, 0, 0, 0)
#define MFMA32(a, b, c) __builtin_amdgcn_mfma_f32_32x32x16_bf16(a, b, c, 0, 0, 0)
#define MFMA16K16(a, b, c) __builtin_amdgcn_mfma_f32_16x16x16bf16_1k(a, b, c, 0, 0, 0)

DEV void cexp_l(float er, float ai, float l, float& re, float& im) {
    float e = __builtin_amdgcn_exp2f(er * l);
    float p = ai * l;
    const float c_hi = 0.15915494309189535f;
    const float c_lo = (float)(0.15915494309189533577 - (double)0.15915494309189535f);
    float t = p * c_hi;
    float err = __builtin_fmaf(p, c_hi, -t) + p * c_lo;
    float fr = (t - __builtin_rintf(t)) + err;
    re = e * __builtin_amdgcn_cosf(fr);
    im = e * __builtin_amdgcn_sinf(fr);
}

DEV float hival(int row) {
    int v;
    if (row < 16) v = (row & 7) + 128 * (row >> 3);
    else if (row < 24) { int i = row - 16; v = 256 + 2 * (i & 3) + 128 * (i >> 2); }
    else if (row < 32) { int i = row - 24; v = 512 + 4 * (i & 1) + 128 * (i >> 1); }
    else if (row < 40) v = 1024 + 128 * (row - 32);
    else if (row == 40) v = 2048;
    else v = 0;
    return (float)v;
}
DEV int kq_index(int l) {
    int q = (int)rbf((float)l);
    int row, lo;
    if (q < 256) { lo = (q >> 3) & 15; row = (q & 7) + 8 * (q >> 7); }
    else if (q < 512) { int r = q - 256; lo = (r >> 3) & 15; row = 16 + ((r & 7) >> 1) + 4 * (r >> 7); }
    else if (q < 1024) { int r = q - 512; lo = (r >> 3) & 15; row = 24 + ((r & 7) >> 2) + 2 * (r >> 7); }
    else if (q < 2048) { int r = q - 1024; lo = (r >> 3) & 15; row = 32 + (r >> 7); }
    else { row = 40; lo = 0; }
    return row * 16 + lo;
}

DEV f2_t ln_scalars(float mean, float var) {
    float mb = rbf(mean);
    float vb = rbf(var);
    float sb = rbf(__builtin_sqrtf(rbf(vb + 1e-5f)));
    f2_t r = {mb, 1.0f / sb};
    return r;
}

struct PrepArgs {
    const u16 *x, *Are, *Aim, *Bre, *Bim, *Cre, *Cim, *logdt, *wqkv, *wo;
    char* ws;
};

__global__ __launch_bounds__(256) void k_prep(PrepArgs a) {
    __shared__ __attribute__((aligned(16))) char smem[36 * 1024];
    const int tid = threadIdx.x, lane = tid & 63, w = tid >> 6;
    const int b = blockIdx.x;
    if (b < 256) {
        float* kq = (float*)smem;
        const int chl = w >> 1, kh = w & 1, ch = 2 * b + chl;
        const int row = lane & 15, g = lane >> 4;
        const float dt = rbf(expf(bf2f(a.logdt[ch])));
        const float lo = (float)(8 * row);
        float hv[3];
#pragma unroll
        for (int mt = 0; mt < 3; ++mt) hv[mt] = hival(16 * mt + row);
        f32x4 acc[3];
#pragma unroll
        for (int mt = 0; mt < 3; ++mt) acc[mt] = f32x4{0.f, 0.f, 0.f, 0.f};
#pragma unroll 1
        for (int ks = 0; ks < 4; ++ks) {
            const int n0 = ch * NM + 64 * kh + 16 * ks + 4 * g;
            u32x2 vAr = *(const u32x2*)(a.Are + n0), vAi = *(const u32x2*)(a.Aim + n0);
            u32x2 vBr = *(const u32x2*)(a.Bre + n0), vBi = *(const u32x2*)(a.Bim + n0);
            u32x2 vCr = *(const u32x2*)(a.Cre + n0), vCi = *(const u32x2*)(a.Cim + n0);
            u32x4 Bh, Bl, Ah[3], Al[3];
#pragma unroll
            for (int i = 0; i < 4; ++i) {
                const int wd = i >> 1;
                const bool hi = i & 1;
                float Ar = hi ? bfhi(vAr[wd]) : bflo(vAr[wd]), Ai = hi ? bfhi(vAi[wd]) : bflo(vAi[wd]);
                float Br = hi ? bfhi(vBr[wd]) : bflo(vBr[wd]), Bi = hi ? bfhi(vBi[wd]) : bflo(vBi[wd]);
                float Cr = hi ? bfhi(vCr[wd]) : bflo(vCr[wd]), Ci = hi ? bfhi(vCi[wd]) : bflo(vCi[wd]);
                float er = (dt * Ar) * 1.4426950408889634f, ai = dt * Ai;
                float dBr = dt * Br, dBi = dt * Bi;
                float cbr = Cr * dBr - Ci * dBi, cbi = Cr * dBi + Ci * dBr;
                float re, im;
                cexp_l(er, ai, lo, re, im);
                unsigned h2 = pk(re, im);
                Bh[i] = h2;
                Bl[i] = pk(re - bflo(h2), im - bfhi(h2));
#pragma unroll
                for (int mt = 0; mt < 3; ++mt) {
                    cexp_l(er, ai, hv[mt], re, im);
                    float gr = cbr * re - cbi * im, gi = -(cbr * im + cbi * re);
                    unsigned g2 = pk(gr, gi);
                    Ah[mt][i] = g2;
                    Al[mt][i] = pk(gr - bflo(g2), gi - bfhi(g2));
                }
            }
            bf16x8 bh = __builtin_bit_cast(bf16x8, Bh), bl = __builtin_bit_cast(bf16x8, Bl);
#pragma unroll
            for (int mt = 0; mt < 3; ++mt) {
                bf16x8 ah = __builtin_bit_cast(bf16x8, Ah[mt]), al = __builtin_bit_cast(bf16x8, Al[mt]);
                acc[mt] = MFMA16(al, bh, acc[mt]);
                acc[mt] = MFMA16(ah, bl, acc[mt]);
                acc[mt] = MFMA16(ah, bh, acc[mt]);
            }
        }
#pragma unroll
        for (int mt = 0; mt < 3; ++mt)
#pragma unroll
            for (int r = 0; r < 4; ++r) kq[(chl * 2 + kh) * 768 + (16 * mt + 4 * g + r) * 16 + row] = acc[mt][r];
        __syncthreads();
        u16* R = (u16*)(a.ws + OFF_R);
        for (int task = tid; task < 2 * 260; task += 256) {
            const int c = task / 260, ci = task % 260;
            float v[8];
#pragma unroll
            for (int j = 0; j < 8; ++j) {
                int l = 2047 - (8 * ci + j);
                int idx = kq_index(l < 0 ? 0 : l);
                float val = kq[(c * 2) * 768 + idx] + kq[(c * 2 + 1) * 768 + idx];
                v[j] = l < 0 ? 0.f : val;
            }
            u32x4 o = {pk(v[0], v[1]), pk(v[2], v[3]), pk(v[4], v[5]), pk(v[6], v[7])};
            *(u32x4*)(R + (size_t)(2 * b + c) * RROW + 8 * ci) = o;
        }
    } else if (b < 384) {
        const int t0 = (b - 256) * 16;
        u16* tile = (u16*)smem;
        f2_t* st = (f2_t*)(a.ws + OFF_ST0);
#pragma unroll
        for (int i = 0; i < 4; ++i) {
            const int tl = 4 * w + i;
            u32x4 v = *(const u32x4*)(a.x + (size_t)(t0 + tl) * D + 8 * lane);
            *(u32x4*)(tile + tl * 520 + 8 * lane) = v;
            float f[8];
#pragma unroll
            for (int j = 0; j < 4; ++j) { f[2 * j] = bflo(v[j]); f[2 * j + 1] = bfhi(v[j]); }
            float s = 0.f;
#pragma unroll
            for (int j = 0; j < 8; ++j) s += f[j];
#pragma unroll
            for (int o = 1; o < 64; o <<= 1) s += __shfl_xor(s, o);
            float mean = s * (1.f / 512.f);
            float q = 0.f;
#pragma unroll
            for (int j = 0; j < 8; ++j) { float d = f[j] - mean; q += d * d; }
#pragma unroll
            for (int o = 1; o < 64; o <<= 1) q += __shfl_xor(q, o);
            if (lane == 0) st[t0 + tl] = ln_scalars(mean, q * (1.f / 512.f));
        }
        __syncthreads();
        u16* xt = (u16*)(a.ws + OFF_XT0);
#pragma unroll
        for (int i = 0; i < 2; ++i) {
            const int d = tid + 256 * i;
            unsigned o[8];
#pragma unroll
            for (int j = 0; j < 8; ++j) o[j] = (unsigned)tile[(2 * j) * 520 + d] | ((unsigned)tile[(2 * j + 1) * 520 + d] << 16);
            u32x4 o0 = {o[0], o[1], o[2], o[3]}, o1 = {o[4], o[5], o[6], o[7]};
            *(u32x4*)(xt + (size_t)d * T + t0) = o0;
            *(u32x4*)(xt + (size_t)d * T + t0 + 8) = o1;
        }
    } else if (b < 448) {
        const int sidx = b - 384;
        const bool isq = sidx < 48;
        const u16* W = isq ? a.wqkv : a.wo;
        const int ldw = isq ? 1536 : 512;
        const int n0 = 32 * (isq ? sidx : sidx - 48);
        u16* tile = (u16*)smem;
#pragma unroll
        for (int it = 0; it < 8; ++it) {
            const int k = (tid >> 2) + 64 * it, c = tid & 3;
            u32x4 v = *(const u32x4*)(W + (size_t)k * ldw + n0 + 8 * c);
            unsigned* dst = (unsigned*)(tile + k * 34 + 8 * c);
            dst[0] = v[0]; dst[1] = v[1]; dst[2] = v[2]; dst[3] = v[3];
        }
        __syncthreads();
        const int np = lane & 15, g = lane >> 4;
#pragma unroll 1
        for (int f = w; f < 32; f += 4) {
            const int nt2 = f >> 4, ks = f & 15;
            unsigned o[4];
#pragma unroll
            for (int j = 0; j < 4; ++j) {
                const int k = 32 * ks + 8 * g + 2 * j;
                o[j] = (unsigned)tile[k * 34 + 16 * nt2 + np] | ((unsigned)tile[(k + 1) * 34 + 16 * nt2 + np] << 16);
            }
            u32x4 ov = {o[0], o[1], o[2], o[3]};
            const int n16 = (n0 >> 4) + nt2;
            size_t off;
            if (isq) {
                const int s = n16 >> 5, h = n16 & 31;
                off = OFF_WF + ((size_t)((h * 3 + s) * 16 + ks) * 64 + lane) * 16;
            } else {
                off = OFF_WOF + ((size_t)(n16 * 16 + ks) * 64 + lane) * 16;
            }
            *(u32x4*)(a.ws + off) = ov;
        }
        if (isq) {
            const int n = tid & 31, part = tid >> 5;
            float s = 0.f;
            for (int k = 64 * part; k < 64 * part + 64; ++k) s += bf2f(tile[k * 34 + n]);
            float* red = (float*)(smem + 512 * 68);
            red[part * 32 + n] = s;
            __syncthreads();
            if (tid < 32) {
                float tot = 0.f;
#pragma unroll
                for (int p = 0; p < 8; ++p) tot += red[p * 32 + tid];
                ((float*)(a.ws + OFF_CS))[n0 + tid] = tot;
            }
        }
    } else {
        u32x4 z = {0u, 0u, 0u, 0u};
        u32x4* p = (u32x4*)(a.ws + OFF_ACC);
        for (int i = tid; i < 2048; i += 256) p[i] = z;
    }
}

constexpr int CV_RC = 4160;
constexpr int CV_RCH = 8 * CV_RC;
constexpr int CV_U0 = 2 * CV_RCH;
constexpr int CV_UPL = 1536, CV_UCH = 4 * CV_UPL;
constexpr int CV_Y0 = CV_U0 + 2 * CV_UCH;
constexpr int CV_YP = 64 * 144;
constexpr int CV_LDS = CV_Y0 + 4 * CV_YP;

template <bool FIRST>
__global__ __launch_bounds__(256) void k_conv(const u16* __restrict__ xTin, u16* __restrict__ xTout,
                                              const u16* __restrict__ Dskip, const char* __restrict__ ws) {
    extern __shared__ __attribute__((aligned(16))) char smem[];
    const int tid = threadIdx.x, lane = tid & 63, w = tid >> 6;
    const int b = blockIdx.x;
    float mb[8], rs[8];
    if (FIRST) {
        const f32x4* st = (const f32x4*)(ws + OFF_ST0) + 4 * tid;
#pragma unroll
        for (int j = 0; j < 4; ++j) { f32x4 v = st[j]; mb[2 * j] = v[0]; rs[2 * j] = v[1]; mb[2 * j + 1] = v[2]; rs[2 * j + 1] = v[3]; }
    } else {
        const long long* ac = (const long long*)(ws + OFF_ACC) + 16 * tid;
#pragma unroll
        for (int j = 0; j < 8; ++j) {
            double mean = (double)ac[2 * j] * (1.0 / (1073741824.0 * 512.0));
            double e2 = (double)ac[2 * j + 1] * (1.0 / (16777216.0 * 512.0));
            double var = e2 - mean * mean;
            f2_t r = ln_scalars((float)mean, (float)(var < 0.0 ? 0.0 : var));
            mb[j] = r[0]; rs[j] = r[1];
        }
    }
    u32x4 xv[2], uv[2];
#pragma unroll
    for (int c = 0; c < 2; ++c) {
        xv[c] = *(const u32x4*)(xTin + (size_t)(2 * b + c) * T + 8 * tid);
#pragma unroll
        for (int j = 0; j < 4; ++j) {
            float u0 = rbf(bflo(xv[c][j]) - mb[2 * j]) * rs[2 * j];
            float u1 = rbf(bfhi(xv[c][j]) - mb[2 * j + 1]) * rs[2 * j + 1];
            uv[c][j] = pk(u0, u1);
        }
        *(u32x4*)(smem + CV_U0 + c * CV_UCH + (tid & 3) * CV_UPL + 16 * (32 + (tid >> 2))) = uv[c];
    }
    {
        u32x4 z = {0u, 0u, 0u, 0u};
        *(u32x4*)(smem + CV_U0 + (tid >> 5) * CV_UPL + 16 * (tid & 31)) = z;
    }
    const u16* R = (const u16*)(ws + OFF_R);
    for (int task = tid; task < 2 * 259; task += 256) {
        const int c = task / 259, ci = task % 259;
        const u32x4* src = (const u32x4*)(R + (size_t)(2 * b + c) * RROW + 8 * ci);
        u32x4 v0 = src[0], v1 = src[1];
        unsigned dw[8] = {v0[0], v0[1], v0[2], v0[3], v1[0], v1[1], v1[2], v1[3]};
        char* dst = smem + c * CV_RCH + 16 * ci;
#pragma unroll
        for (int s = 0; s < 8; ++s) {
            u32x4 o;
#pragma unroll
            for (int d = 0; d < 4; ++d) {
                const int k = (s >> 1) + d;
                o[d] = (s & 1) ? __builtin_amdgcn_alignbyte(dw[k + 1], dw[k], 2) : dw[k];
            }
            *(u32x4*)(dst + s * CV_RC) = o;
        }
    }
    __syncthreads();
    {
        const int c = w >> 1, kk = w & 1;
        const int r = lane & 31, h = lane >> 5;
        const int rho = r >> 3, cc = r & 7;
        const char* pa = smem + c * CV_RCH + (7 - cc) * CV_RC + 16 * (255 + 2 * kk - rho + h);
        const char* pb = smem + CV_U0 + c * CV_UCH + (2 * kk + h) * CV_UPL + 16 * (32 + r);
        f32x16 acc0, acc1;
#pragma unroll
        for (int i = 0; i < 16; ++i) { acc0[i] = 0.f; acc1[i] = 0.f; }
#pragma unroll 4
        for (int dl = 0; dl < 32; ++dl) {
            bf16x8 a0 = *(const bf16x8*)(pa - 64 * dl);
            bf16x8 a1 = *(const bf16x8*)(pa - 64 * dl - 2048);
            bf16x8 b0 = *(const bf16x8*)(pb - 16 * dl);
            bf16x8 b1 = *(const bf16x8*)(pb - 16 * dl + 512);
            acc0 = MFMA32(a0, b0, acc0);
            acc1 = MFMA32(a0, b1, acc1);
            acc1 = MFMA32(a1, b0, acc1);
        }
        char* yp = smem + CV_Y0 + w * CV_YP + r * 144 + 16 * h;
#pragma unroll
        for (int q = 0; q < 4; ++q) {
            f32x4 v0 = {acc0[4 * q], acc0[4 * q + 1], acc0[4 * q + 2], acc0[4 * q + 3]};
            f32x4 v1 = {acc1[4 * q], acc1[4 * q + 1], acc1[4 * q + 2], acc1[4 * q + 3]};
            *(f32x4*)(yp + 32 * q) = v0;
            *(f32x4*)(yp + 32 * 144 + 32 * q) = v1;
        }
    }
    __syncthreads();
#pragma unroll
    for (int c = 0; c < 2; ++c) {
        const char* y0 = smem + CV_Y0 + (2 * c) * CV_YP + (tid >> 2) * 144 + (tid & 3) * 32;
        f32x4 p0 = *(const f32x4*)(y0), p1 = *(const f32x4*)(y0 + 16);
        f32x4 q0 = *(const f32x4*)(y0 + CV_YP), q1 = *(const f32x4*)(y0 + CV_YP + 16);
        float y[8];
#pragma unroll
        for (int j = 0; j < 4; ++j) { y[j] = p0[j] + q0[j]; y[4 + j] = p1[j] + q1[j]; }
        const float dk = bf2f(Dskip[2 * b + c]);
        u32x4 o;
#pragma unroll
        for (int j = 0; j < 4; ++j) {
            float s0 = rbf(rbf(y[2 * j]) + rbf(bflo(uv[c][j]) * dk));
            float s1 = rbf(rbf(y[2 * j + 1]) + rbf(bfhi(uv[c][j]) * dk));
            o[j] = pk(bflo(xv[c][j]) + s0, bfhi(xv[c][j]) + s1);
        }
        *(u32x4*)(xTout + (size_t)(2 * b + c) * T + 8 * tid) = o;
    }
}

constexpr int QK_LDS = 65536 + 4 * 64 * 8 + 64 * 8;
__global__ __launch_bounds__(512) void k_qkv(const u16* __restrict__ xT, char* __restrict__ ws) {
    extern __shared__ __attribute__((aligned(16))) char smem[];
    const int tid = threadIdx.x, lane = tid & 63;
    const int w = __builtin_amdgcn_readfirstlane(tid >> 6);
    const int tt = blockIdx.x >> 3, hg = blockIdx.x & 7;
    const int t0 = 64 * tt;
    {
        const int rr = lane >> 3, chp = lane & 7;
#pragma unroll
        for (int i = 0; i < 8; ++i) {
            const int p = w + 8 * i;
            const int d = 8 * p + rr;
            const int f = ((d >> 1) & 1) | (((d >> 3) & 1) << 1);
            const u16* src = xT + (size_t)d * T + t0 + 8 * (chp ^ (2 * f));
            __builtin_amdgcn_global_load_lds((const unsigned*)src, (__attribute__((address_space(3))) unsigned*)(smem + 1024 * p), 16, 0, 0);
        }
    }
    float* part = (float*)(smem + 65536);
    f2_t* stat = (f2_t*)(smem + 65536 + 2048);
    const int g = lane >> 4, l15 = lane & 15;
    if (w < 4) {
        const int h = 4 * hg + w;
        const bf16x8* wf = (const bf16x8*)(ws + OFF_WF) + (size_t)(h * 3) * 16 * 64 + lane;
        bf16x8 Wf[3][16];
#pragma unroll
        for (int ks = 0; ks < 16; ++ks)
#pragma unroll
            for (int s = 0; s < 3; ++s) Wf[s][ks] = wf[(s * 16 + ks) * 64];
        const float* cs = (const float*)(ws + OFF_CS);
        f32x4 csq = *(const f32x4*)(cs + h * 16 + 4 * g);
        f32x4 csk = *(const f32x4*)(cs + 512 + h * 16 + 4 * g);
        float csv = cs[1024 + h * 16 + l15];
        asm volatile("s_waitcnt vmcnt(0)" ::: "memory");
        __syncthreads();
        f32x4 acc[3][4];
#pragma unroll
        for (int s = 0; s < 3; ++s)
#pragma unroll
            for (int i = 0; i < 4; ++i) acc[s][i] = f32x4{0.f, 0.f, 0.f, 0.f};
        const int q = l15 >> 2, p = lane & 3;
        const int f = ((q >> 1) & 1) | ((g & 1) << 1);
        const lds_char* lbase = (const lds_char*)smem + (8 * g + q) * 128 + 8 * (p & 1);
#pragma unroll
        for (int ks = 0; ks < 16; ++ks) {
#pragma unroll
            for (int ti = 0; ti < 4; ++ti) {
                const int ch = (2 * ti + (p >> 1)) ^ (2 * f);
                typedef short v4s __attribute__((ext_vector_type(4)));
                v4s x0 = __builtin_amdgcn_ds_read_tr16_b64_v4i16((__attribute__((address_space(3))) v4s*)(lbase + ks * 4096 + 16 * ch));
                v4s x1 = __builtin_amdgcn_ds_read_tr16_b64_v4i16((__attribute__((address_space(3))) v4s*)(lbase + ks * 4096 + 512 + 16 * ch));
                bf16x8 xf = {x0[0], x0[1], x0[2], x0[3], x1[0], x1[1], x1[2], x1[3]};
                acc[0][ti] = MFMA16(Wf[0][ks], xf, acc[0][ti]);
                acc[1][ti] = MFMA16(Wf[1][ks], xf, acc[1][ti]);
                acc[2][ti] = MFMA16(xf, Wf[2][ks], acc[2][ti]);
            }
        }
        __syncthreads();
        __syncthreads();
        const float qs = 0.25f * 1.4426950408889634f;
        char* QF = ws + OFF_QF;
        char* KF = ws + OFF_KF;
        char* VF = ws + OFF_VF;
#pragma unroll
        for (int ti = 0; ti < 4; ++ti) {
            f2_t st = stat[16 * ti + l15];
            const int tile = 4 * tt + ti;
            float qv[4], kv[4], vv[4];
#pragma unroll
            for (int r = 0; r < 4; ++r) {
                qv[r] = rbf((acc[0][ti][r] - st[0] * csq[r]) * st[1]) * qs;
                kv[r] = (acc[1][ti][r] - st[0] * csk[r]) * st[1];
                f2_t sv = stat[16 * ti + 4 * g + r];
                vv[r] = (acc[2][ti][r] - sv[0] * csv) * sv[1];
            }
            u32x2 oq = {pk(qv[0], qv[1]), pk(qv[2], qv[3])};
            u32x2 ok = {pk(kv[0], kv[1]), pk(kv[2], kv[3])};
            u32x2 ov = {pk(vv[0], vv[1]), pk(vv[2], vv[3])};
            *(u32x2*)(QF + ((size_t)(h * 128 + tile) * 64 + lane) * 8) = oq;
            *(u32x2*)(KF + ((size_t)(h * 128 + tile) * 64 + lane) * 8) = ok;
            *(u32x2*)(VF + ((size_t)(h * 64 + (tile >> 1)) * 64 + lane) * 16 + 8 * (tile & 1)) = ov;
        }
    } else {
        asm volatile("s_waitcnt vmcnt(0)" ::: "memory");
        __syncthreads();
        const int wq = w - 4;
        const int rr = lane >> 3, chk = lane & 7;
        float s1[8], s2[8];
#pragma unroll
        for (int j = 0; j < 8; ++j) { s1[j] = 0.f; s2[j] = 0.f; }
#pragma unroll 4
        for (int it = 0; it < 16; ++it) {
            const int d = 128 * wq + 8 * it + rr;
            const int f = ((d >> 1) & 1) | (((d >> 3) & 1) << 1);
            u32x4 v = *(const u32x4*)(smem + d * 128 + 16 * (chk ^ (2 * f)));
#pragma unroll
            for (int j = 0; j < 4; ++j) {
                float a0 = bflo(v[j]), a1 = bfhi(v[j]);
                s1[2 * j] += a0; s2[2 * j] += a0 * a0;
                s1[2 * j + 1] += a1; s2[2 * j + 1] += a1 * a1;
            }
        }
#pragma unroll
        for (int j = 0; j < 8; ++j)
#pragma unroll
            for (int o = 8; o < 64; o <<= 1) { s1[j] += __shfl_xor(s1[j], o); s2[j] += __shfl_xor(s2[j], o); }
        if (lane < 8) {
#pragma unroll
            for (int j = 0; j < 8; ++j) { part[(wq * 64 + 8 * chk + j) * 2] = s1[j]; part[(wq * 64 + 8 * chk + j) * 2 + 1] = s2[j]; }
        }
        __syncthreads();
        if (wq == 0) {
            float a = 0.f, bb = 0.f;
#pragma unroll
            for (int i = 0; i < 4; ++i) { a += part[(i * 64 + lane) * 2]; bb += part[(i * 64 + lane) * 2 + 1]; }
            float mean = a * (1.f / 512.f);
            float var = bb * (1.f / 512.f) - mean * mean;
            stat[lane] = ln_scalars(mean, var < 0.f ? 0.f : var);
        }
        __syncthreads();
    }
}

__global__ __launch_bounds__(512) void k_attn(char* __restrict__ ws) {
    const int tid = threadIdx.x, lane = tid & 63;
    const int w = __builtin_amdgcn_readfirstlane(tid >> 6);
    const int tt = blockIdx.x >> 3, hg = blockIdx.x & 7;
    const int h = 4 * hg + (w & 3);
    const int a = 2 * tt + (w >> 2);
    const int g = lane >> 4, l15 = lane & 15;
    const bf16x4* QF = (const bf16x4*)(ws + OFF_QF) + (size_t)h * 128 * 64 + lane;
    const bf16x4* KF = (const bf16x4*)(ws + OFF_KF) + (size_t)h * 128 * 64 + lane;
    const bf16x8* VF = (const bf16x8*)(ws + OFF_VF) + (size_t)h * 64 * 64 + lane;
    bf16x4 Kf[18];
    bf16x8 Vf[9];
    bf16x4 Qf[2];
    Qf[0] = QF[(2 * a) * 64];
    Qf[1] = QF[(2 * a + 1) * 64];
#pragma unroll
    for (int i = 0; i < 18; ++i) { int kt = 2 * a - 16 + i; Kf[i] = KF[(kt < 0 ? 0 : kt) * 64]; }
#pragma unroll
    for (int j = 0; j < 9; ++j) { int vt = a - 8 + j; Vf[j] = VF[(vt < 0 ? 0 : vt) * 64]; }
    const float NEG = -__builtin_inff();
#pragma unroll
    for (int e = 0; e < 2; ++e) {
        f32x4 S[17];
#pragma unroll
        for (int i = 0; i < 17; ++i) {
            S[i] = MFMA16K16(Kf[i + e], Qf[e], (f32x4{0.f, 0.f, 0.f, 0.f}));
        }
#pragma unroll
        for (int r = 0; r < 4; ++r) {
            if (!(4 * g + r > l15)) S[0][r] = NEG;
            if (4 * g + r > l15) S[16][r] = NEG;
        }
#pragma unroll
        for (int i = 0; i < 16; ++i) {
            if (2 * a - 16 + i + e < 0) S[i] = f32x4{NEG, NEG, NEG, NEG};
        }
        float m = NEG;
#pragma unroll
        for (int i = 0; i < 17; ++i) m = fmaxf(fmaxf(m, fmaxf(S[i][0], S[i][1])), fmaxf(S[i][2], S[i][3]));
        m = fmaxf(m, __shfl_xor(m, 16));
        m = fmaxf(m, __shfl_xor(m, 32));
        float l = 0.f;
        u32x2 P[18];
#pragma unroll
        for (int i = 0; i < 17; ++i) {
            float p0 = __builtin_amdgcn_exp2f(S[i][0] - m), p1 = __builtin_amdgcn_exp2f(S[i][1] - m);
            float p2 = __builtin_amdgcn_exp2f(S[i][2] - m), p3 = __builtin_amdgcn_exp2f(S[i][3] - m);
            l += (p0 + p1) + (p2 + p3);
            P[i + e] = u32x2{pk(p0, p1), pk(p2, p3)};
        }
        P[e == 0 ? 17 : 0] = u32x2{0u, 0u};
        l += __shfl_xor(l, 16);
        l += __shfl_xor(l, 32);
        f32x4 O = {0.f, 0.f, 0.f, 0.f};
#pragma unroll
        for (int j = 0; j < 9; ++j) {
            u32x4 pb = {P[2 * j][0], P[2 * j][1], P[2 * j + 1][0], P[2 * j + 1][1]};
            O = MFMA16(Vf[j], __builtin_bit_cast(bf16x8, pb), O);
        }
        const float inv = 1.0f / l;
        u32x2 o = {pk(O[0] * inv, O[1] * inv), pk(O[2] * inv, O[3] * inv)};
        const int tile = 2 * a + e;
        const int gp = 2 * (h & 1) + (g >> 1);
        *(u32x2*)(ws + OFF_OF + ((size_t)(tile * 16 + (h >> 1)) * 64 + l15 + 16 * gp) * 16 + 8 * (g & 1)) = o;
    }
}

constexpr int OP_ROW = 68 * 4;
template <bool LAST>
__global__ __launch_bounds__(512) void k_oproj(const u16* __restrict__ xTin, u16* __restrict__ xTout, u16* __restrict__ out,
                                               char* __restrict__ ws) {
    __shared__ __attribute__((aligned(16))) char smem[2 * 64 * OP_ROW + 8 * 64 * 8];
    const int tid = threadIdx.x, lane = tid & 63;
    const int w = __builtin_amdgcn_readfirstlane(tid >> 6);
    const int tt = blockIdx.x >> 3, nb = blockIdx.x & 7;
    const int ti = w & 3, kh = w >> 2;
    const int g = lane >> 4, l15 = lane & 15;
    const bf16x8* wf = (const bf16x8*)(ws + OFF_WOF) + (size_t)(4 * nb) * 16 * 64 + (8 * kh) * 64 + lane;
    const bf16x8* of = (const bf16x8*)(ws + OFF_OF) + (size_t)((4 * tt + ti) * 16 + 8 * kh) * 64 + lane;
    bf16x8 Wf[4][8], Of[8];
#pragma unroll
    for (int ks = 0; ks < 8; ++ks) {
        Of[ks] = of[ks * 64];
#pragma unroll
        for (int nt = 0; nt < 4; ++nt) Wf[nt][ks] = wf[(nt * 16 + ks) * 64];
    }
    const int n = tid >> 3, tc = tid & 7;
    u32x4 xr = *(const u32x4*)(xTin + (size_t)(64 * nb + n) * T + 64 * tt + 8 * tc);
    f32x4 acc[4];
#pragma unroll
    for (int nt = 0; nt < 4; ++nt) acc[nt] = f32x4{0.f, 0.f, 0.f, 0.f};
#pragma unroll
    for (int ks = 0; ks < 8; ++ks)
#pragma unroll
        for (int nt = 0; nt < 4; ++nt) acc[nt] = MFMA16(Of[ks], Wf[nt][ks], acc[nt]);
#pragma unroll
    for (int nt = 0; nt < 4; ++nt)
        *(f32x4*)(smem + (kh * 64 + 16 * nt + l15) * OP_ROW + (16 * ti + 4 * g) * 4) = acc[nt];
    __syncthreads();
    float v[8];
    {
        const char* p0 = smem + n * OP_ROW + tc * 32;
        f32x4 a0 = *(const f32x4*)(p0), a1 = *(const f32x4*)(p0 + 16);
        f32x4 b0 = *(const f32x4*)(p0 + 64 * OP_ROW), b1 = *(const f32x4*)(p0 + 64 * OP_ROW + 16);
#pragma unroll
        for (int j = 0; j < 4; ++j) { v[j] = a0[j] + b0[j]; v[4 + j] = a1[j] + b1[j]; }
    }
    u32x4 o;
#pragma unroll
    for (int j = 0; j < 4; ++j) o[j] = pk(bflo(xr[j]) + rbf(v[2 * j]), bfhi(xr[j]) + rbf(v[2 * j + 1]));
    if (!LAST) {
        *(u32x4*)(xTout + (size_t)(64 * nb + n) * T + 64 * tt + 8 * tc) = o;
        float s1[8], s2[8];
#pragma unroll
        for (int j = 0; j < 4; ++j) {
            float a0 = bflo(o[j]), a1 = bfhi(o[j]);
            s1[2 * j] = a0; s2[2 * j] = a0 * a0; s1[2 * j + 1] = a1; s2[2 * j + 1] = a1 * a1;
        }
#pragma unroll
        for (int j = 0; j < 8; ++j)
#pragma unroll
            for (int of2 = 8; of2 < 64; of2 <<= 1) { s1[j] += __shfl_xor(s1[j], of2); s2[j] += __shfl_xor(s2[j], of2); }
        float* part = (float*)(smem + 2 * 64 * OP_ROW);
        if (lane < 8) {
#pragma unroll
            for (int j = 0; j < 8; ++j) { part[(w * 64 + 8 * tc + j) * 2] = s1[j]; part[(w * 64 + 8 * tc + j) * 2 + 1] = s2[j]; }
        }
        __syncthreads();
        if (tid < 128) {
            float a = 0.f;
#pragma unroll
            for (int i = 0; i < 8; ++i) a += part[i * 128 + tid];
            const float sc = (tid & 1) ? 16777216.0f : 1073741824.0f;
            long long iv = (long long)__builtin_rintf(a * sc);
            atomicAdd((unsigned long long*)(ws + OFF_ACC) + (size_t)(64 * tt) * 2 + tid, (unsigned long long)iv);
        }
    } else {
        __syncthreads();
        u16* img = (u16*)smem;
#pragma unroll
        for (int j = 0; j < 4; ++j) {
            img[(8 * tc + 2 * j) * 72 + n] = (u16)(o[j] & 0xffff);
            img[(8 * tc + 2 * j + 1) * 72 + n] = (u16)(o[j] >> 16);
        }
        __syncthreads();
        const int t = tid >> 3, nc = tid & 7;
        u32x4 r = *(const u32x4*)(img + t * 72 + 8 * nc);
        *(u32x4*)(out + (size_t)(64 * tt + t) * D + 64 * nb + 8 * nc) = r;
    }
}

extern "C" void kernel_launch(void* const* d_in, const int* in_sizes, int n_in, void* d_out, int out_size, void* d_ws,
                              size_t ws_size, hipStream_t stream) {
    char* ws = (char*)d_ws;
    PrepArgs pa;
    pa.x = (const u16*)d_in[0];
    pa.Are = (const u16*)d_in[1];
    pa.Aim = (const u16*)d_in[2];
    pa.Bre = (const u16*)d_in[3];
    pa.Bim = (const u16*)d_in[4];
    pa.Cre = (const u16*)d_in[5];
    pa.Cim = (const u16*)d_in[6];
    pa.logdt = (const u16*)d_in[7];
    pa.wqkv = (const u16*)d_in[10];
    pa.wo = (const u16*)d_in[11];
    pa.ws = ws;
    const u16* Dskip = (const u16*)d_in[8];
    u16* xt0 = (u16*)(ws + OFF_XT0);
    u16* xt1 = (u16*)(ws + OFF_XT1);
    u16* xt2 = (u16*)(ws + OFF_XT2);
    u16* xt3 = (u16*)(ws + OFF_XT3);
    (void)hipFuncSetAttribute((const void*)k_conv<true>, hipFuncAttributeMaxDynamicSharedMemorySize, CV_LDS);
    (void)hipFuncSetAttribute((const void*)k_conv<false>, hipFuncAttributeMaxDynamicSharedMemorySize, CV_LDS);
    (void)hipFuncSetAttribute((const void*)k_qkv, hipFuncAttributeMaxDynamicSharedMemorySize, QK_LDS);
    k_prep<<<449, 256, 0, stream>>>(pa);
    k_conv<true><<<256, 256, CV_LDS, stream>>>(xt0, xt1, Dskip, ws);
    k_qkv<<<256, 512, QK_LDS, stream>>>(xt1, ws);
    k_attn<<<256, 512, 0, stream>>>(ws);
    k_oproj<false><<<256, 512, 0, stream>>>(xt1, xt2, (u16*)d_out, ws);
    k_conv<false><<<256, 256, CV_LDS, stream>>>(xt2, xt3, Dskip, ws);
    k_qkv<<<256, 512, QK_LDS, stream>>>(xt3, ws);
    k_attn<<<256, 512, 0, stream>>>(ws);
    k_oproj<true><<<256, 512, 0, stream>>>(xt3, nullptr, (u16*)d_out, ws);
}
```

```cpp
#include <hip/hip_runtime.h>
#include <hip/hip_bf16.h>
#include <stdint.h>

typedef short bf16x8 __attribute__((ext_vector_type(8)));
typedef short bf16x4 __attribute__((ext_vector_type(4)));
typedef float f32x4 __attribute__((ext_vector_type(4)));
typedef float f32x16 __attribute__((ext_vector_type(16)));
typedef unsigned u32x4 __attribute__((ext_vector_type(4)));
typedef unsigned u32x2 __attribute__((ext_vector_type(2)));
typedef __bf16 bf2_t __attribute__((ext_vector_type(2)));
typedef float f2_t __attribute__((ext_vector_type(2)));
typedef unsigned short u16;
typedef __attribute__((address_space(3))) char lds_char;
typedef __attribute__((address_space(3))) unsigned lds_u32;

#define DEV __device__ __forceinline__
#define STAMP(i) do {} while (0)

#define PST(i) do {} while (0)
constexpr int T = 2048, D = 512, NM = 128;

constexpr unsigned MB = 1024 * 1024;
constexpr unsigned OFF_XT0 = 0, OFF_XT1 = 2 * MB, OFF_XT2 = 4 * MB, OFF_XT3 = 6 * MB;
constexpr unsigned OFF_WF = 8 * MB;
constexpr unsigned OFF_WOF = 9 * MB + 512 * 1024;
constexpr unsigned OFF_CS = 10 * MB;
constexpr unsigned OFF_ST0 = 10 * MB + 64 * 1024;
constexpr unsigned OFF_ACC = 10 * MB + 128 * 1024;
constexpr unsigned OFF_FLAGS = 10 * MB + 192 * 1024;
constexpr unsigned OFF_KV = 12 * MB;
constexpr unsigned OFF_OF = 20 * MB;
constexpr unsigned OFF_R = 24 * MB;
constexpr unsigned WS_USED = 28 * MB;

DEV float bf2f(unsigned v) { return __uint_as_float(v << 16); }
DEV float bflo(unsigned v) { return __uint_as_float(v << 16); }
DEV float bfhi(unsigned v) { return __uint_as_float(v & 0xffff0000u); }
DEV unsigned pk(float lo, float hi) {
    f2_t v = {lo, hi};
    return __builtin_bit_cast(unsigned, __builtin_convertvector(v, bf2_t));
}
DEV float rbf(float x) { return __uint_as_float(pk(x, 0.f) << 16); }
DEV float dot2(unsigned a, unsigned b, float acc) {
    return __builtin_amdgcn_fdot2_f32_bf16(__builtin_bit_cast(bf2_t, a), __builtin_bit_cast(bf2_t, b), acc, false);
}
DEV float dpp_ror8(float x) { return __builtin_bit_cast(float, __builtin_amdgcn_mov_dpp(__builtin_bit_cast(int, x), 0x128, 0xf, 0xf, true)); }
DEV float xrow16_max(float x) {
    auto s = __builtin_amdgcn_permlane16_swap(__float_as_uint(x), __float_as_uint(x), false, false);
    x = fmaxf(__uint_as_float(s[0]), __uint_as_float(s[1]));
    auto t = __builtin_amdgcn_permlane32_swap(__float_as_uint(x), __float_as_uint(x), false, false);
    return fmaxf(__uint_as_float(t[0]), __uint_as_float(t[1]));
}
DEV float xrow16_sum(float x) {
    auto s = __builtin_amdgcn_permlane16_swap(__float_as_uint(x), __float_as_uint(x), false, false);
    x = __uint_as_float(s[0]) + __uint_as_float(s[1]);
    auto t = __builtin_amdgcn_permlane32_swap(__float_as_uint(x), __float_as_uint(x), false, false);
    return __uint_as_float(t[0]) + __uint_as_float(t[1]);
}

#define MFMA16(a, b, c) __builtin_amdgcn_mfma_f32_16x16x32_bf16(a, b, c, 0, 0, 0)
#define MFMA32(a, b, c) __builtin_amdgcn_mfma_f32_32x32x16_bf16(a, b, c, 0, 0, 0)
#define MFMA16K16(a, b, c) __builtin_amdgcn_mfma_f32_16x16x16bf16_1k(a, b, c, 0, 0, 0)
#define SC1 16
#define LD128(rs, off) __builtin_amdgcn_raw_buffer_load_b128(rs, off, 0, SC1)
#define ST128(v, rs, off) __builtin_amdgcn_raw_buffer_store_b128(v, rs, off, 0, SC1)
#define VM0() asm volatile("s_waitcnt vmcnt(0)" ::: "memory")
typedef short v4s __attribute__((ext_vector_type(4)));
#define TRREAD(p) __builtin_amdgcn_ds_read_tr16_b64_v4i16((__attribute__((address_space(3))) v4s*)(p))
#define SB() __builtin_amdgcn_sched_barrier(0)
#define PHASE_IDS() int tid = tid0; asm volatile("" : "+v"(tid)); const int lane = tid & 63, g = lane >> 4, l15 = lane & 15; (void)g; (void)l15;

DEV f2_t cexp_l(f2_t ef, float l) {
    f2_t t = ef * l;
    float e = __builtin_amdgcn_exp2f(t[0]);
    float fr = __builtin_amdgcn_fractf(t[1]);
    f2_t cs = {__builtin_amdgcn_cosf(fr), __builtin_amdgcn_sinf(fr)};
    return cs * e;
}
DEV void split2(f2_t v, unsigned& hi, unsigned& lo) {
    hi = pk(v[0], v[1]);
    f2_t hf = {bflo(hi), bfhi(hi)};
    f2_t r = v - hf;
    lo = pk(r[0], r[1]);
}
DEV float hival(int row) {
    int v;
    if (row < 16) v = (row & 7) + 128 * (row >> 3);
    else if (row < 24) { int i = row - 16; v = 256 + 2 * (i & 3) + 128 * (i >> 2); }
    else if (row < 32) { int i = row - 24; v = 512 + 4 * (i & 1) + 128 * (i >> 1); }
    else if (row < 40) v = 1024 + 128 * (row - 32);
    else if (row == 40) v = 2048;
    else v = 0;
    return (float)v;
}
DEV f2_t ln_scalars(float mean, float var) {
    float mb = rbf(mean);
    float vb = rbf(var);
    float sb = rbf(__builtin_amdgcn_sqrtf(rbf(vb + 1e-5f)));
    f2_t r = {mb, __builtin_amdgcn_rcpf(sb)};
    return r;
}

template <int CTRL> DEV float dppf(float x) { return __builtin_bit_cast(float, __builtin_amdgcn_mov_dpp(__builtin_bit_cast(int, x), CTRL, 0xf, 0xf, true)); }
DEV float row16_sum(float x) {
    x += dppf<0xB1>(x);
    x += dppf<0x4E>(x);
    x += dppf<0x124>(x);
    x += dppf<0x128>(x);
    return x;
}

struct PrepArgs {
    const u16 *x, *wqkv, *wo, *Are, *Aim, *Bre, *Bim, *Cre, *Cim, *logdt;
    char* ws;
};
__global__ __launch_bounds__(512) void k_prep(PrepArgs a) {
    char* __restrict__ ws = a.ws;
    __shared__ __attribute__((aligned(16))) char smem[8 * 648 * 4 + 2 * 648 * 4 + 8 * 1088 + 128 * 80 + 1024];
    constexpr int P_X = 10 * 648 * 4, P_W = P_X + 8 * 1088, P_CS = P_W + 128 * 80;
    const int tid = threadIdx.x, lane = tid & 63, w = tid >> 6;
    const int b = blockIdx.x;
    const int g = lane >> 4, l15 = lane & 15;
    PST(0);
    const int chl = w >> 2, kq4 = w & 3, ch = 2 * b + chl;
    u32x2 vAr[2], vAi[2], vBr[2], vBi[2], vCr[2], vCi[2];
#pragma unroll
    for (int ks = 0; ks < 2; ++ks) {
        const int n0 = ch * NM + 32 * kq4 + 16 * ks + 4 * g;
        vAr[ks] = *(const u32x2*)(a.Are + n0); vAi[ks] = *(const u32x2*)(a.Aim + n0);
        vBr[ks] = *(const u32x2*)(a.Bre + n0); vBi[ks] = *(const u32x2*)(a.Bim + n0);
        vCr[ks] = *(const u32x2*)(a.Cre + n0); vCi[ks] = *(const u32x2*)(a.Cim + n0);
    }
    const float ldt = bf2f(a.logdt[ch]);
    const u32x4 xrow = *(const u32x4*)(a.x + (size_t)(8 * b + w) * D + 8 * lane);
    const bool isq = b < 192;
    const int id2 = isq ? b : b - 192;
    const int kq = id2 & 3, nb = id2 >> 2;
    const u32x4 wv = *(const u32x4*)((isq ? a.wqkv : a.wo) + (size_t)(128 * kq + (tid >> 2)) * (isq ? 1536 : 512) + 32 * nb + 8 * (tid & 3));
    {
        u32x4 z = {0u, 0u, 0u, 0u};
        if (tid < 8) ((u32x4*)(ws + OFF_ACC))[b * 8 + tid] = z;
        else if (tid < 18) ((u32x4*)(ws + OFF_FLAGS))[b * 10 + tid - 8] = z;
    }
    {
        const float dt = rbf(expf(ldt));
        PST(1);
        const float lo = (float)(8 * l15);
        float hv[3];
#pragma unroll
        for (int mt = 0; mt < 3; ++mt) hv[mt] = hival(16 * mt + l15);
        f32x4 acc[3];
#pragma unroll
        for (int mt = 0; mt < 3; ++mt) acc[mt] = f32x4{0.f, 0.f, 0.f, 0.f};
#pragma unroll
        for (int ks = 0; ks < 2; ++ks) {
            u32x4 Bh, Bl, Ah[3], Al[3];
#pragma unroll
            for (int i = 0; i < 4; ++i) {
                const int wd = i >> 1;
                const bool hi = i & 1;
                float Ar = hi ? bfhi(vAr[ks][wd]) : bflo(vAr[ks][wd]), Ai = hi ? bfhi(vAi[ks][wd]) : bflo(vAi[ks][wd]);
                float Br = hi ? bfhi(vBr[ks][wd]) : bflo(vBr[ks][wd]), Bi = hi ? bfhi(vBi[ks][wd]) : bflo(vBi[ks][wd]);
                float Cr = hi ? bfhi(vCr[ks][wd]) : bflo(vCr[ks][wd]), Ci = hi ? bfhi(vCi[ks][wd]) : bflo(vCi[ks][wd]);
                const f2_t ef = {(dt * Ar) * 1.4426950408889634f, (dt * Ai) * 0.15915494309189535f};
                float dBr = dt * Br, dBi = dt * Bi;
                float cbr = Cr * dBr - Ci * dBi, cbi = Cr * dBi + Ci * dBr;
                f2_t e0 = cexp_l(ef, lo);
                f2_t gg = {cbr * e0[0] - cbi * e0[1], -(cbr * e0[1] + cbi * e0[0])};
                unsigned hh, ll;
                split2(gg, hh, ll);
                Bh[i] = hh; Bl[i] = ll;
#pragma unroll
                for (int mt = 0; mt < 3; ++mt) {
                    split2(cexp_l(ef, hv[mt]), hh, ll);
                    Ah[mt][i] = hh; Al[mt][i] = ll;
                }
            }
            bf16x8 bh = __builtin_bit_cast(bf16x8, Bh), bl = __builtin_bit_cast(bf16x8, Bl);
#pragma unroll
            for (int mt = 0; mt < 3; ++mt) {
                bf16x8 ah = __builtin_bit_cast(bf16x8, Ah[mt]), al = __builtin_bit_cast(bf16x8, Al[mt]);
                acc[mt] = MFMA16(al, bh, acc[mt]);
                acc[mt] = MFMA16(ah, bl, acc[mt]);
                acc[mt] = MFMA16(ah, bh, acc[mt]);
            }
        }
        PST(2);
        float* kw = (float*)smem + w * 648;
        const int p0 = 4 * (g & 1) + 128 * (g >> 1) + 8 * l15;
        *(f32x4*)(kw + p0) = acc[0];
        if (g < 2) *(f32x4*)(kw + 256 + 64 * g + 4 * l15) = acc[1];
        else {
            const int pb = 384 + 64 * (g - 2) + 2 * l15;
            *(f2_t*)(kw + pb) = f2_t{acc[1][0], acc[1][1]};
            *(f2_t*)(kw + pb + 32) = f2_t{acc[1][2], acc[1][3]};
        }
        if (g < 2) {
#pragma unroll
            for (int r = 0; r < 4; ++r) kw[512 + 16 * (4 * g + r) + l15] = acc[2][r];
        } else if (lane == 32) kw[640] = acc[2][0];
    }
    {
        *(u32x4*)(smem + P_X + w * 1088 + 16 * lane) = xrow;
        *(u32x4*)(smem + P_W + (tid >> 2) * 80 + 16 * (tid & 3)) = wv;
        float s = 0.f;
#pragma unroll
        for (int j = 0; j < 4; ++j) s = dot2(xrow[j], 0x3f803f80u, s);
        s = xrow16_sum(row16_sum(s));
        const float mean = s * (1.f / 512.f);
        float q = 0.f;
#pragma unroll
        for (int j = 0; j < 4; ++j) { float d0 = bflo(xrow[j]) - mean, d1 = bfhi(xrow[j]) - mean; q += d0 * d0 + d1 * d1; }
        q = xrow16_sum(row16_sum(q));
        if (lane == 0) ((f2_t*)(ws + OFF_ST0))[8 * b + w] = ln_scalars(mean, q * (1.f / 512.f));
    }
    __syncthreads();
    PST(3);
    {
        const float* kq_ = (const float*)smem;
        float* kt = (float*)smem + 8 * 648;
        for (int i = tid; i < 2 * 648; i += 512) {
            const int c = i >= 648, pi = i - 648 * c;
            const float* kp = kq_ + c * (4 * 648) + pi;
            kt[i] = (kp[0] + kp[648]) + (kp[1296] + kp[1944]);
        }
        const int q = l15 >> 2, p = lane & 3;
        {
            const int d0 = 64 * w + 16 * g;
            const lds_char* src = (const lds_char*)smem + P_X + q * 1088 + (d0 + 4 * p) * 2;
            u32x2 r0 = __builtin_bit_cast(u32x2, TRREAD(src)), r1 = __builtin_bit_cast(u32x2, TRREAD(src + 4 * 1088));
            *(u32x4*)(ws + OFF_XT0 + (size_t)(d0 + l15) * (T * 2) + 16 * b) = u32x4{r0[0], r0[1], r1[0], r1[1]};
        }
        {
            const int nt2 = w & 1, k4 = w >> 1;
            const lds_char* src = (const lds_char*)smem + P_W + (32 * k4 + 8 * g + q) * 80 + (16 * nt2 + 4 * p) * 2;
            u32x2 r0 = __builtin_bit_cast(u32x2, TRREAD(src)), r1 = __builtin_bit_cast(u32x2, TRREAD(src + 4 * 80));
            u32x4 ov = {r0[0], r0[1], r1[0], r1[1]};
            const int n16 = 2 * nb + nt2, ks = 4 * kq + k4;
            size_t off;
            if (isq) off = OFF_WF + ((size_t)(((n16 & 31) * 3 + (n16 >> 5)) * 16 + ks) * 64 + lane) * 16;
            else off = OFF_WOF + ((size_t)(n16 * 16 + ks) * 64 + lane) * 16;
            *(u32x4*)(ws + off) = ov;
            float cs = 0.f;
#pragma unroll
            for (int j = 0; j < 4; ++j) cs = dot2(ov[j], 0x3f803f80u, cs);
            cs = xrow16_sum(cs);
            if (g == 0) ((float*)(smem + P_CS))[w * 16 + l15] = cs;
        }
        __syncthreads();
        if (isq && tid < 32) {
            const float* cp = (const float*)(smem + P_CS) + tid;
            ((float*)(ws + OFF_CS))[kq * 1536 + 32 * nb + tid] = (cp[0] + cp[32]) + (cp[64] + cp[96]);
        }
        for (int task = tid; task < 2 * 260; task += 512) {
            const int c = task >= 260, ci = task - 260 * c;
            const int m = 255 - ci;
            int base; unsigned offs;
            if (m < 32) { base = 8 * m; offs = 0x76543210u; }
            else if (m < 64) { base = 256 + 4 * (m - 32); offs = 0x43222100u; }
            else if (m < 128) { base = 384 + 2 * (m - 64); offs = 0x22111000u; }
            else { base = 512 + (m - 128); offs = 0x11100000u | ((unsigned)(m & 1) << 16); }
            if (m < 0) base = 0;
            float v[8];
#pragma unroll
            for (int j = 0; j < 8; ++j) {
                const int i = 7 - j;
                float val = kt[c * 648 + base + ((offs >> (4 * i)) & 15)];
                v[j] = m < 0 ? 0.f : val;
            }
            u32x4 o = {pk(v[0], v[1]), pk(v[2], v[3]), pk(v[4], v[5]), pk(v[6], v[7])};
            *(u32x4*)(ws + OFF_R + (size_t)(2 * b + c) * 4160 + 16 * ci) = o;
        }
    }
    PST(4);
}

constexpr int CV_RC = 4160, CV_RCH = 8 * CV_RC;
constexpr int CV_U0 = 2 * CV_RCH, CV_UPL = 1536, CV_UCH = 4 * CV_UPL;
constexpr int CV_Y0 = CV_U0 + 2 * CV_UCH;
constexpr int CV_YP = 64 * 144;
constexpr int Q_XCH = 81920;
constexpr int Q_PART = Q_XCH + 49152;
constexpr int A_ONES = Q_PART + 4096;
constexpr int PF_SCR = A_ONES + 20480;
constexpr int MEGA_LDS = PF_SCR + 8192;
static_assert(MEGA_LDS <= 163840 && CV_Y0 + 8 * CV_YP <= PF_SCR, "lds");

struct MegaArgs {
    const u16* Dskip;
    u16* out;
    char* ws;
};

#define ALOAD(p) __hip_atomic_load((p), __ATOMIC_RELAXED, __HIP_MEMORY_SCOPE_AGENT)
#define ASTORE(p, v) __hip_atomic_store((p), (v), __ATOMIC_RELAXED, __HIP_MEMORY_SCOPE_AGENT)
#define AADD(p, v) __hip_atomic_fetch_add((p), (v), __ATOMIC_RELAXED, __HIP_MEMORY_SCOPE_AGENT)
constexpr unsigned SPIN_MAX = 4000000u;
DEV void signal_flag(unsigned* flags, int fid, unsigned step) {
    VM0();
    __syncthreads();
    if (threadIdx.x == 0) ASTORE(flags + fid * 32, step);
}
DEV void wait_words(unsigned* flags, int idx, unsigned step) {
    if (threadIdx.x < 64) {
        for (unsigned spins = 0; spins < SPIN_MAX; ++spins) {
            unsigned v = step;
            if (idx >= 0) v = ALOAD(flags + idx * 32);
            if (__all(v >= step)) break;
            __builtin_amdgcn_s_sleep(1);
        }
    }
    asm volatile("" ::: "memory");
    __syncthreads();
}
DEV void bar_arrive(unsigned* flags, int b, unsigned epoch) {
    VM0();
    __syncthreads();
    if (threadIdx.x < 64) {
        int last = 0;
        if (threadIdx.x == 0) last = (AADD(flags + 8192 + (b & 15) * 32, 1u) == 16u * epoch - 1u);
        last = __builtin_amdgcn_readfirstlane(last);
        if (last && threadIdx.x < 8) ASTORE(flags + 9280 + threadIdx.x * 32 + (b & 15), epoch);
    }
}
DEV void bar_wait(unsigned* flags, int b, unsigned epoch) {
    if (threadIdx.x < 64) {
        for (unsigned spins = 0; spins < SPIN_MAX; ++spins) {
            unsigned v = epoch;
            if (threadIdx.x < 16) v = ALOAD(flags + 9280 + ((b >> 3) & 7) * 32 + threadIdx.x);
            if (__all(v >= epoch)) break;
            __builtin_amdgcn_s_sleep(1);
        }
    }
    asm volatile("" ::: "memory");
    __syncthreads();
}
DEV float max3f(float a, float b, float c) { return __builtin_fmaxf(__builtin_fmaxf(a, b), c); }
DEV float max2f(float a, float b) { return __builtin_fmaxf(a, b); }
template <bool EDGE>
DEV u32x4 attn_body(const char* smem, int hw, int kh, int lane, int ab, const bf16x8 Qf) {
    const int r32 = lane & 31, hh = lane >> 5;
    const float NEG = -1.0e30f;
    const char* kb = smem + hw * 20480 + kh * 2048 + lane * 16;
    const char* vb = (r32 < 16 ? smem + hw * 20480 + kh * 2048 + 1024 : smem + A_ONES) + hh * 256 + (r32 & 15) * 16;
    bf16x8 Kf[9];
#pragma unroll
    for (int j = 0; j < 9; ++j) Kf[j] = *(const bf16x8*)(kb + j * 2048);
    f32x16 zero;
#pragma unroll
    for (int i = 0; i < 16; ++i) zero[i] = 0.f;
    float m0 = NEG, m1 = NEG;
#pragma unroll
    for (int j = 0; j < 9; ++j) {
        f32x16 S = MFMA32(Kf[j], Qf, zero);
        if (j == 0) {
#pragma unroll
            for (int i = 0; i < 16; ++i) if (!((i & 3) + 8 * (i >> 2) + 4 * hh > r32)) S[i] = NEG;
        }
        if (j == 8) {
#pragma unroll
            for (int i = 0; i < 16; ++i) if ((i & 3) + 8 * (i >> 2) + 4 * hh > r32) S[i] = NEG;
        }
        float a0 = m0, a1 = m1;
#pragma unroll
        for (int i = 0; i < 16; i += 4) { a0 = max3f(a0, S[i], S[i + 1]); a1 = max3f(a1, S[i + 2], S[i + 3]); }
        if (EDGE) { if (ab - 8 + j >= 0) { m0 = a0; m1 = a1; } }
        else { m0 = a0; m1 = a1; }
    }
    float m = max2f(m0, m1);
    {
        auto t = __builtin_amdgcn_permlane32_swap(__float_as_uint(m), __float_as_uint(m), false, false);
        m = max2f(__uint_as_float(t[0]), __uint_as_float(t[1]));
    }
    f32x16 nm;
#pragma unroll
    for (int i = 0; i < 16; ++i) nm[i] = -m;
    f32x16 O = zero;
#pragma unroll
    for (int j = 0; j < 9; ++j) {
        bf16x8 v0 = *(const bf16x8*)(vb + j * 2048), v1 = *(const bf16x8*)(vb + j * 2048 + 512);
        f32x16 S = MFMA32(Kf[j], Qf, nm);
        if (j == 0) {
#pragma unroll
            for (int i = 0; i < 16; ++i) if (!((i & 3) + 8 * (i >> 2) + 4 * hh > r32)) S[i] = NEG;
        }
        if (j == 8) {
#pragma unroll
            for (int i = 0; i < 16; ++i) if ((i & 3) + 8 * (i >> 2) + 4 * hh > r32) S[i] = NEG;
        }
        u32x4 p0, p1;
#pragma unroll
        for (int i = 0; i < 4; ++i) {
            p0[i] = pk(__builtin_amdgcn_exp2f(S[2 * i]), __builtin_amdgcn_exp2f(S[2 * i + 1]));
            p1[i] = pk(__builtin_amdgcn_exp2f(S[8 + 2 * i]), __builtin_amdgcn_exp2f(S[8 + 2 * i + 1]));
        }
        if (!EDGE || ab - 8 + j >= 0) {
            O = MFMA32(v0, __builtin_bit_cast(bf16x8, p0), O);
            O = MFMA32(v1, __builtin_bit_cast(bf16x8, p1), O);
        }
    }
    const float inv = __builtin_amdgcn_rcpf(O[8]);
    const unsigned a0 = pk(O[0] * inv, O[1] * inv), a1 = pk(O[2] * inv, O[3] * inv);
    const unsigned b0 = pk(O[4] * inv, O[5] * inv), b1 = pk(O[6] * inv, O[7] * inv);
    auto sx = __builtin_amdgcn_permlane32_swap(a0, b0, false, false);
    auto sy = __builtin_amdgcn_permlane32_swap(a1, b1, false, false);
    return u32x4{sx[0], sy[0], sx[1], sy[1]};
}

template <int layer>
DEV void layer_body(const MegaArgs& a, char* smem) {
    char* ws = a.ws;
    const int tid0 = threadIdx.x;
    const int w = __builtin_amdgcn_readfirstlane(tid0 >> 6);
    const int b = blockIdx.x;
    const int tt = 4 * (b & 7) + ((b >> 3) & 3), hg = b >> 5;
    const int fid = tt * 8 + hg;
    const int hw = w & 3, kh = w >> 2;
    const int h = 4 * hg + hw;
    unsigned* flags = (unsigned*)(ws + OFF_FLAGS);
    const __amdgpu_buffer_rsrc_t rs = __builtin_amdgcn_make_buffer_rsrc((void*)ws, 0, WS_USED, 0x00020000);
    {
        const unsigned xin = layer == 0 ? OFF_XT0 : OFF_XT2;
        const unsigned xmid = layer == 0 ? OFF_XT1 : OFF_XT3;
        const unsigned kvb = OFF_KV + layer * 4 * MB;
        const unsigned ofb = OFF_OF + layer * 2 * MB;
        const unsigned step0 = 4 * layer;
        {
            PHASE_IDS();
            const int c = tid >> 8, ti = tid & 255;
            u32x4 xv, sv[4], s0v[2];
            if (layer == 0) {
                xv = *(const u32x4*)(ws + xin + (size_t)(2 * b + c) * (T * 2) + 16 * ti);
                s0v[0] = *(const u32x4*)(ws + OFF_ST0 + 32 * tid);
                s0v[1] = *(const u32x4*)(ws + OFF_ST0 + 32 * tid + 16);
            }
            const float dk = bf2f(a.Dskip[2 * b + c]);
            {
                const int ci2 = 256 + (tid & 3), c2 = (tid >> 2) & 1;
                const u32x4* src = (const u32x4*)(ws + OFF_R + (size_t)(2 * b + c) * 4160 + 16 * ti);
                const u32x4* src2 = (const u32x4*)(ws + OFF_R + (size_t)(2 * b + c2) * 4160 + 16 * (ci2 > 258 ? 258 : ci2));
                u32x4 v0 = src[0], v1 = src[1], z0 = src2[0], z1 = src2[1];
#pragma unroll
                for (int rep = 0; rep < 2; ++rep) {
                    if (rep == 1 && tid >= 8) break;
                    unsigned dw[8];
#pragma unroll
                    for (int j = 0; j < 4; ++j) { dw[j] = rep ? z0[j] : v0[j]; dw[4 + j] = rep ? z1[j] : v1[j]; }
                    char* dst = smem + (rep ? c2 * CV_RCH + 16 * (ci2 > 258 ? 258 : ci2) : c * CV_RCH + 16 * ti);
#pragma unroll
                    for (int s = 0; s < 8; ++s) {
                        u32x4 o;
#pragma unroll
                        for (int d = 0; d < 4; ++d) {
                            const int k = (s >> 1) + d;
                            o[d] = (s & 1) ? __builtin_amdgcn_alignbyte(dw[k + 1], dw[k], 2) : dw[k];
                        }
                        *(u32x4*)(dst + s * CV_RC) = o;
                    }
                }
                if (tid < 256) *(u32x4*)(smem + CV_U0 + (tid >> 5) * CV_UPL + 16 * (tid & 31)) = u32x4{0u, 0u, 0u, 0u};
            }
            STAMP(16 * layer + 1);
            f32x4* stl = (f32x4*)(smem + CV_Y0);
            if (layer != 0) {
                bar_wait(flags, b, 2);
                STAMP(16 + 2);
                xv = LD128(rs, xin + (unsigned)(2 * b + c) * (T * 2) + 16 * ti);
                u32x4 av[4];
#pragma unroll
                for (int j = 0; j < 4; ++j) av[j] = LD128(rs, OFF_ACC + 8192 * j + 16 * tid);
#pragma unroll
                for (int j = 0; j < 4; ++j) {
                    double s1 = (double)(int)av[j][1] * 4294967296.0 + (double)av[j][0];
                    double s2 = (double)(int)av[j][3] * 4294967296.0 + (double)av[j][2];
                    double mean = s1 * (1.0 / (1073741824.0 * 512.0));
                    double var = s2 * (1.0 / (16777216.0 * 512.0)) - mean * mean;
                    ((f2_t*)stl)[512 * j + tid] = ln_scalars((float)mean, fmaxf((float)var, 0.f));
                }
            } else {
                stl[2 * tid] = __builtin_bit_cast(f32x4, s0v[0]);
                stl[2 * tid + 1] = __builtin_bit_cast(f32x4, s0v[1]);
            }
            __syncthreads();
#pragma unroll
            for (int j = 0; j < 4; ++j) sv[j] = __builtin_bit_cast(u32x4, stl[4 * ti + j]);
            u32x4 uv;
#pragma unroll
            for (int j = 0; j < 4; ++j) {
                float u0 = rbf(bflo(xv[j]) - __uint_as_float(sv[j][0])) * __uint_as_float(sv[j][1]);
                float u1 = rbf(bfhi(xv[j]) - __uint_as_float(sv[j][2])) * __uint_as_float(sv[j][3]);
                uv[j] = pk(u0, u1);
            }
            *(u32x4*)(smem + CV_U0 + c * CV_UCH + (ti & 3) * CV_UPL + 16 * (32 + (ti >> 2))) = uv;
            __syncthreads();
            STAMP(16 * layer + 3);
            if (layer == 0) {
                const char* src = ws + OFF_WF + (size_t)(b >> 3) * 65536 + w * 8192 + lane * 16;
#pragma unroll
                for (int i = 0; i < 8; ++i)
                    __builtin_amdgcn_global_load_lds((const unsigned*)(src + i * 1024), (lds_u32*)(smem + PF_SCR + w * 1024), 16, 0, 0);
            }
            {
                const int cw = w >> 2, kk = w & 1, par = (w >> 1) & 1;
                const int r = lane & 31, hh = lane >> 5;
                const int rho = r >> 3, cc = r & 7;
                const char* pa = smem + cw * CV_RCH + (7 - cc) * CV_RC + 16 * (255 + 2 * kk - rho + hh) - 64 * par;
                const char* pb = smem + CV_U0 + cw * CV_UCH + (2 * kk + hh) * CV_UPL + 16 * (32 + r) - 16 * par;
                f32x16 acc0, acc1;
#pragma unroll
                for (int i = 0; i < 16; ++i) { acc0[i] = 0.f; acc1[i] = 0.f; }
                bf16x8 a0 = *(const bf16x8*)(pa), a1 = *(const bf16x8*)(pa - 2048);
                bf16x8 b0 = *(const bf16x8*)(pb), b1 = *(const bf16x8*)(pb + 512);
#pragma unroll
                for (int it = 0; it < 16; ++it) {
                    bf16x8 na0, na1, nb0, nb1;
                    if (it < 15) {
                        na0 = *(const bf16x8*)(pa - 128 * (it + 1));
                        na1 = *(const bf16x8*)(pa - 128 * (it + 1) - 2048);
                        nb0 = *(const bf16x8*)(pb - 32 * (it + 1));
                        nb1 = *(const bf16x8*)(pb - 32 * (it + 1) + 512);
                    }
                    SB();
                    acc0 = MFMA32(a0, b0, acc0);
                    acc1 = MFMA32(a0, b1, acc1);
                    acc1 = MFMA32(a1, b0, acc1);
                    SB();
                    if (it < 15) { a0 = na0; a1 = na1; b0 = nb0; b1 = nb1; }
                }
                char* yp = smem + CV_Y0 + w * CV_YP + r * 144 + 16 * hh;
#pragma unroll
                for (int q = 0; q < 4; ++q) {
                    f32x4 v0 = {acc0[4 * q], acc0[4 * q + 1], acc0[4 * q + 2], acc0[4 * q + 3]};
                    f32x4 v1 = {acc1[4 * q], acc1[4 * q + 1], acc1[4 * q + 2], acc1[4 * q + 3]};
                    *(f32x4*)(yp + 32 * q) = v0;
                    *(f32x4*)(yp + 32 * 144 + 32 * q) = v1;
                }
            }
            STAMP(16 * layer + 4);
            __syncthreads();
            {
                const char* y0 = smem + CV_Y0 + (4 * c) * CV_YP + (ti >> 2) * 144 + (ti & 3) * 32;
                f32x4 pp[8];
#pragma unroll
                for (int p = 0; p < 4; ++p) { pp[2 * p] = *(const f32x4*)(y0 + p * CV_YP); pp[2 * p + 1] = *(const f32x4*)(y0 + p * CV_YP + 16); }
                f32x4 ya = (pp[0] + pp[2]) + (pp[4] + pp[6]), yb = (pp[1] + pp[3]) + (pp[5] + pp[7]);
                float y[8] = {ya[0], ya[1], ya[2], ya[3], yb[0], yb[1], yb[2], yb[3]};
                u32x4 o;
#pragma unroll
                for (int j = 0; j < 4; ++j) {
                    float s0 = rbf(rbf(y[2 * j]) + rbf(bflo(uv[j]) * dk));
                    float s1 = rbf(rbf(y[2 * j + 1]) + rbf(bfhi(uv[j]) * dk));
                    o[j] = pk(bflo(xv[j]) + s0, bfhi(xv[j]) + s1);
                }
                ST128(o, rs, xmid + (unsigned)(2 * b + c) * (T * 2) + 16 * ti);
            }
            bar_arrive(flags, b, 2 * layer + 1);
            STAMP(16 * layer + 5);
        }
        u32x2 xres[2];
        bf16x8 Qf;
        {
            PHASE_IDS();
            bf16x8 Wf[3][8];
            f32x4 csq = {0.f, 0.f, 0.f, 0.f}, csk = {0.f, 0.f, 0.f, 0.f};
            float csv = 0.f;
            {
                const bf16x8* wf = (const bf16x8*)(ws + OFF_WF) + (size_t)(h * 3) * 16 * 64 + (8 * kh) * 64 + lane;
#pragma unroll
                for (int i = 0; i < 8; ++i)
#pragma unroll
                    for (int s = 0; s < 3; ++s) Wf[s][i] = wf[(s * 16 + ((i + hw) & 7)) * 64];
                const float* cs = (const float*)(ws + OFF_CS);
#pragma unroll
                for (int kq = 0; kq < 4; ++kq) {
                    csq += *(const f32x4*)(cs + kq * 1536 + h * 16 + 4 * g);
                    csk += *(const f32x4*)(cs + kq * 1536 + 512 + h * 16 + 4 * g);
                    csv += cs[kq * 1536 + 1024 + h * 16 + l15];
                }
            }
            bar_wait(flags, b, 2 * layer + 1);
            STAMP(16 * layer + 6);
            {
                const int rr = lane >> 3, chp = lane & 7;
#pragma unroll
                for (int i = 0; i < 8; ++i) {
                    const int p = w + 8 * i;
                    const int d = 8 * p + rr;
                    const int f = ((d >> 1) & 1) | (((d >> 3) & 1) << 1);
                    const char* src = ws + xmid + (size_t)d * (T * 2) + 128 * tt + 16 * (chp ^ (2 * f));
                    __builtin_amdgcn_global_load_lds((const unsigned*)src, (lds_u32*)(smem + 1024 * p), 16, 0, SC1);
                }
            }
            VM0();
            __syncthreads();
            STAMP(16 * layer + 7);
            {
                const int d = 64 * hg + 16 * hw + l15;
                const int f = ((d >> 1) & 1) | (((d >> 3) & 1) << 1);
#pragma unroll
                for (int i = 0; i < 2; ++i)
                    xres[i] = *(const u32x2*)(smem + d * 128 + 16 * ((4 * kh + 2 * i + (g >> 1)) ^ (2 * f)) + 8 * (g & 1));
            }
            f32x4 acc[3][4];
#pragma unroll
            for (int s = 0; s < 3; ++s)
#pragma unroll
                for (int i = 0; i < 4; ++i) acc[s][i] = f32x4{0.f, 0.f, 0.f, 0.f};
            float s1[4] = {0.f, 0.f, 0.f, 0.f}, s2[4] = {0.f, 0.f, 0.f, 0.f};
            const int q = l15 >> 2, p = lane & 3;
            const int f = ((q >> 1) & 1) | ((g & 1) << 1);
            const lds_char* lbase = (const lds_char*)smem + kh * 32768 + (8 * g + q) * 128 + 8 * (p & 1);
            bf16x8 xf[4], xn[4];
#define TRLD(dst, i_) { const lds_char* lb_ = lbase + (((i_) + hw) & 7) * 4096; _Pragma("unroll") for (int ti = 0; ti < 4; ++ti) { \
                const int ch = (2 * (ti ^ (2 * kh)) + (p >> 1)) ^ (2 * f); \
                v4s x0 = TRREAD(lb_ + 16 * ch); \
                v4s x1 = TRREAD(lb_ + 512 + 16 * ch); \
                dst[ti] = bf16x8{x0[0], x0[1], x0[2], x0[3], x1[0], x1[1], x1[2], x1[3]}; } }
            TRLD(xf, 0);
#pragma unroll
            for (int i = 0; i < 8; ++i) {
                if (i < 7) { TRLD(xn, i + 1); }
                SB();
#pragma unroll
                for (int ti = 0; ti < 4; ++ti) {
                    acc[0][ti] = MFMA16(Wf[0][i], xf[ti], acc[0][ti]);
                    acc[1][ti] = MFMA16(Wf[1][i], xf[ti], acc[1][ti]);
                    acc[2][ti] = MFMA16(xf[ti], Wf[2][i], acc[2][ti]);
                    if ((i & 3) == 0) {
                        u32x4 xu = __builtin_bit_cast(u32x4, xf[ti]);
#pragma unroll
                        for (int j = 0; j < 4; ++j) { s1[ti] = dot2(xu[j], 0x3f803f80u, s1[ti]); s2[ti] = dot2(xu[j], xu[j], s2[ti]); }
                        asm volatile("" : "+v"(s1[ti]), "+v"(s2[ti]));
                    }
                }
                SB();
                if (i < 7) {
#pragma unroll
                    for (int ti = 0; ti < 4; ++ti) xf[ti] = xn[ti];
                }
            }
            f2_t* part = (f2_t*)(smem + Q_PART);
#pragma unroll
            for (int ti = 0; ti < 4; ++ti) {
                float a1 = xrow16_sum(s1[ti]), a2 = xrow16_sum(s2[ti]);
                if (g == ti) part[w * 64 + 16 * (ti ^ (2 * kh)) + l15] = f2_t{a1, a2};
            }
            {
                char* xp = smem + Q_XCH + w * 6144 + lane * 16;
#pragma unroll
                for (int s = 0; s < 3; ++s)
#pragma unroll
                    for (int i = 0; i < 2; ++i) *(f32x4*)(xp + (s * 2 + i) * 1024) = acc[s][2 + i];
            }
            __syncthreads();
            STAMP(16 * layer + 8);
            {
                f2_t st;
                {
                    float x1 = 0.f, x2 = 0.f;
#pragma unroll
                    for (int i = 0; i < 8; ++i) { f2_t v = part[i * 64 + 32 * kh + (lane & 31)]; x1 += v[0]; x2 += v[1]; }
                    float mean = x1 * (1.f / 512.f);
                    float var = x2 * (1.f / 512.f) - mean * mean;
                    st = ln_scalars(mean, fmaxf(var, 0.f));
                }
                const char* xp = smem + Q_XCH + (w ^ 4) * 6144 + lane * 16;
                const float qs = 0.25f * 1.4426950408889634f;
                u32x4 qo, ko, vo;
#pragma unroll
                for (int i = 0; i < 2; ++i) {
                    f32x4 aq = *(const f32x4*)(xp + (0 + i) * 1024), ak = *(const f32x4*)(xp + (2 + i) * 1024), av = *(const f32x4*)(xp + (4 + i) * 1024);
                    aq += acc[0][i];
                    ak += acc[1][i];
                    av += acc[2][i];
                    const float m0 = __shfl(st[0], 16 * i + l15), r0 = __shfl(st[1], 16 * i + l15);
                    float qv[4], kv[4], vv[4];
#pragma unroll
                    for (int r = 0; r < 4; ++r) {
                        qv[r] = rbf((aq[r] - m0 * csq[r]) * r0) * qs;
                        kv[r] = (ak[r] - m0 * csk[r]) * r0;
                        const float m1 = __shfl(st[0], 16 * i + 4 * g + r), r1 = __shfl(st[1], 16 * i + 4 * g + r);
                        vv[r] = (av[r] - m1 * csv) * r1;
                    }
                    qo[2 * i] = pk(qv[0], qv[1]); qo[2 * i + 1] = pk(qv[2], qv[3]);
                    ko[2 * i] = pk(kv[0], kv[1]); ko[2 * i + 1] = pk(kv[2], kv[3]);
                    vo[2 * i] = pk(vv[0], vv[1]); vo[2 * i + 1] = pk(vv[2], vv[3]);
                }
                {
                    auto qx = __builtin_amdgcn_permlane16_swap(qo[0], qo[2], false, false);
                    auto qy = __builtin_amdgcn_permlane16_swap(qo[1], qo[3], false, false);
                    Qf = __builtin_bit_cast(bf16x8, (u32x4{qx[0], qy[0], qx[1], qy[1]}));
                    auto kx = __builtin_amdgcn_permlane16_swap(ko[0], ko[2], false, false);
                    auto ky = __builtin_amdgcn_permlane16_swap(ko[1], ko[3], false, false);
                    ko = u32x4{kx[0], ky[0], kx[1], ky[1]};
                    auto vx = __builtin_amdgcn_permlane32_swap(vo[0], vo[2], false, false);
                    auto vy = __builtin_amdgcn_permlane32_swap(vo[1], vo[3], false, false);
                    vo = u32x4{vx[0], vy[0], vx[1], vy[1]};
                }
                const unsigned go = kvb + (unsigned)(h * 64 + 2 * tt + kh) * 2048 + lane * 16;
                ST128(ko, rs, go);
                ST128(vo, rs, go + 1024);
                char* lo = smem + hw * 20480 + (8 + kh) * 2048 + lane * 16;
                *(u32x4*)(lo) = ko;
                *(u32x4*)(lo + 1024) = vo;
                const u32x4 on = {0x3f803f80u, 0x3f803f80u, 0x3f803f80u, 0x3f803f80u};
                *(u32x4*)(smem + A_ONES + tid * 16) = on;
                *(u32x4*)(smem + A_ONES + 8192 + tid * 16) = on;
                if (tid < 256) *(u32x4*)(smem + A_ONES + 16384 + tid * 16) = on;
            }
            signal_flag(flags, fid, step0 + 2);
            STAMP(16 * layer + 10);
        }
        {
            PHASE_IDS();
            {
                const int pt = tt - 1 - lane;
                wait_words(flags, (lane < 4 && pt >= 0) ? pt * 8 + hg : -1, step0 + 2);
            }
            STAMP(16 * layer + 11);
#pragma unroll
            for (int i = 0; i < 4; ++i) {
                const int s = 4 * kh + i;
                const int P = 2 * tt - 8 + s;
                if (P >= 0) {
                    const char* src = ws + kvb + (size_t)(h * 64 + P) * 2048 + lane * 16;
                    char* dst = smem + hw * 20480 + s * 2048;
                    __builtin_amdgcn_global_load_lds((const unsigned*)src, (lds_u32*)(dst), 16, 0, SC1);
                    __builtin_amdgcn_global_load_lds((const unsigned*)(src + 1024), (lds_u32*)(dst + 1024), 16, 0, SC1);
                }
            }
            VM0();
            __syncthreads();
            STAMP(16 * layer + 12);
            const int ab = 2 * tt + kh;
            u32x4 o = ab < 8 ? attn_body<true>(smem, hw, kh, lane, ab, Qf) : attn_body<false>(smem, hw, kh, lane, ab, Qf);
            {
                const int tile = 2 * ab + (g & 1);
                const int gp = 2 * (h & 1) + (g >> 1);
                ST128(o, rs, ofb + (unsigned)((tile * 16 + (h >> 1)) * 64 + l15 + 16 * gp) * 16);
            }
            STAMP(16 * layer + 9);
            signal_flag(flags, fid, step0 + 3);
            STAMP(16 * layer + 13);
        }
        {
            PHASE_IDS();
            bf16x8 Wo[16];
            {
                const bf16x8* wo = (const bf16x8*)(ws + OFF_WOF) + (size_t)((4 * hg + hw) * 16) * 64 + lane;
#pragma unroll
                for (int ks = 0; ks < 16; ++ks) Wo[ks] = wo[ks * 64];
            }
            wait_words(flags, lane < 8 ? tt * 8 + lane : -1, step0 + 3);
            STAMP(16 * layer + 14);
#pragma unroll
            for (int i = 0; i < 8; ++i) {
                const int p = w + 8 * i;
                const char* src = ws + ofb + (size_t)(4 * tt * 16 + p) * 1024 + lane * 16;
                __builtin_amdgcn_global_load_lds((const unsigned*)src, (lds_u32*)(smem + 1024 * p), 16, 0, SC1);
            }
            VM0();
            __syncthreads();
            f32x4 acc[2][2];
#pragma unroll
            for (int i = 0; i < 2; ++i) { acc[i][0] = f32x4{0.f, 0.f, 0.f, 0.f}; acc[i][1] = f32x4{0.f, 0.f, 0.f, 0.f}; }
            {
                const char* op = smem + (2 * kh) * 16384 + lane * 16;
                bf16x8 of[2][8], og[2][8];
#pragma unroll
                for (int ks = 0; ks < 8; ++ks)
#pragma unroll
                    for (int i = 0; i < 2; ++i) of[i][ks] = *(const bf16x8*)(op + (i * 16 + ks) * 1024);
                SB();
#pragma unroll
                for (int ks = 0; ks < 8; ++ks) {
#pragma unroll
                    for (int i = 0; i < 2; ++i) og[i][ks] = *(const bf16x8*)(op + (i * 16 + 8 + ks) * 1024);
#pragma unroll
                    for (int i = 0; i < 2; ++i) acc[i][ks & 1] = MFMA16(of[i][ks], Wo[ks], acc[i][ks & 1]);
                    SB();
                }
#pragma unroll
                for (int ks = 0; ks < 8; ++ks)
#pragma unroll
                    for (int i = 0; i < 2; ++i) acc[i][ks & 1] = MFMA16(og[i][ks], Wo[8 + ks], acc[i][ks & 1]);
            }
            u32x2 o[2];
#pragma unroll
            for (int i = 0; i < 2; ++i) {
                f32x4 v = acc[i][0] + acc[i][1];
                o[i][0] = pk(bflo(xres[i][0]) + rbf(v[0]), bfhi(xres[i][0]) + rbf(v[1]));
                o[i][1] = pk(bflo(xres[i][1]) + rbf(v[2]), bfhi(xres[i][1]) + rbf(v[3]));
            }
            if (layer == 0) {
                {
                    auto sx = __builtin_amdgcn_permlane16_swap(o[0][0], o[1][0], false, false);
                    auto sy = __builtin_amdgcn_permlane16_swap(o[0][1], o[1][1], false, false);
                    u32x4 ov = {sx[0], sy[0], sx[1], sy[1]};
                    ST128(ov, rs, OFF_XT2 + (unsigned)(64 * hg + 16 * hw + l15) * (T * 2) + 128 * tt + 64 * kh + 32 * (g & 1) + 16 * (g >> 1));
                }
                float sel = 0.f;
#pragma unroll
                for (int i = 0; i < 2; ++i)
#pragma unroll
                    for (int r = 0; r < 4; ++r) {
                        const float x = (r & 1) ? bfhi(o[i][r >> 1]) : bflo(o[i][r >> 1]);
                        const float a1 = row16_sum(x), a2 = row16_sum(x * x);
                        if (l15 == 2 * (4 * i + r)) sel = a1;
                        if (l15 == 2 * (4 * i + r) + 1) sel = a2;
                    }
                {
                    const int e = l15 >> 1;
                    const int t = 64 * tt + 32 * kh + 16 * (e >> 2) + 4 * g + (e & 3);
                    const float sc = (l15 & 1) ? 16777216.0f : 1073741824.0f;
                    long long iv = (long long)__builtin_rintf(sel * sc);
                    atomicAdd((unsigned long long*)(ws + OFF_ACC) + (size_t)t * 2 + (l15 & 1), (unsigned long long)iv);
                }
                bar_arrive(flags, b, 2);
            } else {
                __syncthreads();
                u16* img = (u16*)smem;
#pragma unroll
                for (int i = 0; i < 2; ++i)
#pragma unroll
                    for (int r = 0; r < 4; ++r)
                        img[(32 * kh + 16 * i + 4 * g + r) * 72 + 16 * hw + l15] = (u16)((r & 1) ? (o[i][r >> 1] >> 16) : (o[i][r >> 1] & 0xffff));
                __syncthreads();
                const int t = tid >> 3, nc = tid & 7;
                u32x4 r = *(const u32x4*)(img + t * 72 + 8 * nc);
                *(u32x4*)(a.out + (size_t)(64 * tt + t) * D + 64 * hg + 8 * nc) = r;
            }
            STAMP(16 * layer + 15);
        }
    }
}

__global__ __launch_bounds__(512, 2) void k_mega(MegaArgs a) {
    extern __shared__ __attribute__((aligned(16))) char smem[];
    char* ws = a.ws;
    STAMP(0);
    layer_body<0>(a, smem);
    layer_body<1>(a, smem);
}

extern "C" void kernel_launch(void* const* d_in, const int* in_sizes, int n_in, void* d_out, int out_size, void* d_ws,
                              size_t ws_size, hipStream_t stream) {
    char* ws = (char*)d_ws;
    PrepArgs pa;
    pa.x = (const u16*)d_in[0];
    pa.wqkv = (const u16*)d_in[10];
    pa.wo = (const u16*)d_in[11];
    pa.Are = (const u16*)d_in[1];
    pa.Aim = (const u16*)d_in[2];
    pa.Bre = (const u16*)d_in[3];
    pa.Bim = (const u16*)d_in[4];
    pa.Cre = (const u16*)d_in[5];
    pa.Cim = (const u16*)d_in[6];
    pa.logdt = (const u16*)d_in[7];
    pa.ws = ws;
    MegaArgs ma;
    ma.Dskip = (const u16*)d_in[8];
    ma.out = (u16*)d_out;
    ma.ws = ws;
    (void)hipFuncSetAttribute((const void*)k_mega, hipFuncAttributeMaxDynamicSharedMemorySize, MEGA_LDS);
    k_prep<<<256, 512, 0, stream>>>(pa);
    k_mega<<<256, 512, MEGA_LDS, stream>>>(ma);
}
```

```cpp
#include <hip/hip_runtime.h>
#include <hip/hip_bf16.h>
#include <stdint.h>

typedef short bf16x8 __attribute__((ext_vector_type(8)));
typedef short bf16x4 __attribute__((ext_vector_type(4)));
typedef float f32x4 __attribute__((ext_vector_type(4)));
typedef float f32x16 __attribute__((ext_vector_type(16)));
typedef unsigned u32x4 __attribute__((ext_vector_type(4)));
typedef unsigned u32x2 __attribute__((ext_vector_type(2)));
typedef __bf16 bf2_t __attribute__((ext_vector_type(2)));
typedef float f2_t __attribute__((ext_vector_type(2)));
typedef unsigned short u16;
typedef __attribute__((address_space(3))) char lds_char;
typedef __attribute__((address_space(3))) unsigned lds_u32;

#define DEV __device__ __forceinline__
#define STAMP(i) do {} while (0)

#define PST(i) do {} while (0)
constexpr int T = 2048, D = 512, NM = 128;

constexpr unsigned MB = 1024 * 1024;
constexpr unsigned OFF_XT0 = 0, OFF_XT1 = 2 * MB, OFF_XT2 = 4 * MB, OFF_XT3 = 6 * MB;
constexpr unsigned OFF_WF = 8 * MB;
constexpr unsigned OFF_WOF = 9 * MB + 512 * 1024;
constexpr unsigned OFF_CS = 10 * MB;
constexpr unsigned OFF_ST0 = 10 * MB + 64 * 1024;
constexpr unsigned OFF_ACC = 10 * MB + 128 * 1024;
constexpr unsigned OFF_FLAGS = 10 * MB + 192 * 1024;
constexpr unsigned OFF_KV = 12 * MB;
constexpr unsigned OFF_OF = 20 * MB;
constexpr unsigned OFF_R = 24 * MB;
constexpr unsigned WS_USED = 28 * MB;

DEV float bf2f(unsigned v) { return __uint_as_float(v << 16); }
DEV float bflo(unsigned v) { return __uint_as_float(v << 16); }
DEV float bfhi(unsigned v) { return __uint_as_float(v & 0xffff0000u); }
DEV unsigned pk(float lo, float hi) {
    f2_t v = {lo, hi};
    return __builtin_bit_cast(unsigned, __builtin_convertvector(v, bf2_t));
}
DEV float rbf(float x) { return __uint_as_float(pk(x, 0.f) << 16); }
DEV float dot2(unsigned a, unsigned b, float acc) {
    return __builtin_amdgcn_fdot2_f32_bf16(__builtin_bit_cast(bf2_t, a), __builtin_bit_cast(bf2_t, b), acc, false);
}
DEV float dpp_ror8(float x) { return __builtin_bit_cast(float, __builtin_amdgcn_mov_dpp(__builtin_bit_cast(int, x), 0x128, 0xf, 0xf, true)); }
DEV float xrow16_max(float x) {
    auto s = __builtin_amdgcn_permlane16_swap(__float_as_uint(x), __float_as_uint(x), false, false);
    x = fmaxf(__uint_as_float(s[0]), __uint_as_float(s[1]));
    auto t = __builtin_amdgcn_permlane32_swap(__float_as_uint(x), __float_as_uint(x), false, false);
    return fmaxf(__uint_as_float(t[0]), __uint_as_float(t[1]));
}
DEV float xrow16_sum(float x) {
    auto s = __builtin_amdgcn_permlane16_swap(__float_as_uint(x), __float_as_uint(x), false, false);
    x = __uint_as_float(s[0]) + __uint_as_float(s[1]);
    auto t = __builtin_amdgcn_permlane32_swap(__float_as_uint(x), __float_as_uint(x), false, false);
    return __uint_as_float(t[0]) + __uint_as_float(t[1]);
}

#define MFMA16(a, b, c) __builtin_amdgcn_mfma_f32_16x16x32_bf16(a, b, c, 0, 0, 0)
#define MFMA32(a, b, c) __builtin_amdgcn_mfma_f32_32x32x16_bf16(a, b, c, 0, 0, 0)
#define MFMA16K16(a, b, c) __builtin_amdgcn_mfma_f32_16x16x16bf16_1k(a, b, c, 0, 0, 0)
#define SC1 16
#define LD128(rs, off) __builtin_amdgcn_raw_buffer_load_b128(rs, off, 0, SC1)
#define ST128(v, rs, off) __builtin_amdgcn_raw_buffer_store_b128(v, rs, off, 0, SC1)
#define VM0() asm volatile("s_waitcnt vmcnt(0)" ::: "memory")
typedef short v4s __attribute__((ext_vector_type(4)));
#define TRREAD(p) __builtin_amdgcn_ds_read_tr16_b64_v4i16((__attribute__((address_space(3))) v4s*)(p))
#define SB() __builtin_amdgcn_sched_barrier(0)
#define BAR() asm volatile("s_waitcnt lgkmcnt(0)\n\ts_barrier" ::: "memory")
#define PHASE_IDS() int tid = tid0; asm volatile("" : "+v"(tid)); const int lane = tid & 63, g = lane >> 4, l15 = lane & 15; (void)g; (void)l15;

DEV f2_t cexp_l(f2_t ef, float l) {
    f2_t t = ef * l;
    float e = __builtin_amdgcn_exp2f(t[0]);
    float fr = __builtin_amdgcn_fractf(t[1]);
    f2_t cs = {__builtin_amdgcn_cosf(fr), __builtin_amdgcn_sinf(fr)};
    return cs * e;
}
DEV void split2(f2_t v, unsigned& hi, unsigned& lo) {
    hi = pk(v[0], v[1]);
    f2_t hf = {bflo(hi), bfhi(hi)};
    f2_t r = v - hf;
    lo = pk(r[0], r[1]);
}
DEV float hival(int row) {
    int v;
    if (row < 16) v = (row & 7) + 128 * (row >> 3);
    else if (row < 24) { int i = row - 16; v = 256 + 2 * (i & 3) + 128 * (i >> 2); }
    else if (row < 32) { int i = row - 24; v = 512 + 4 * (i & 1) + 128 * (i >> 1); }
    else if (row < 40) v = 1024 + 128 * (row - 32);
    else if (row == 40) v = 2048;
    else v = 0;
    return (float)v;
}
DEV f2_t ln_scalars(float mean, float var) {
    float mb = rbf(mean);
    float vb = rbf(var);
    float sb = rbf(__builtin_amdgcn_sqrtf(rbf(vb + 1e-5f)));
    f2_t r = {mb, __builtin_amdgcn_rcpf(sb)};
    return r;
}

template <int CTRL> DEV float dppf(float x) { return __builtin_bit_cast(float, __builtin_amdgcn_mov_dpp(__builtin_bit_cast(int, x), CTRL, 0xf, 0xf, true)); }
DEV float row16_sum(float x) {
    x += dppf<0xB1>(x);
    x += dppf<0x4E>(x);
    x += dppf<0x124>(x);
    x += dppf<0x128>(x);
    return x;
}

constexpr unsigned POISON = 0xFFFFFFFFu;
struct PrepArgs {
    const u16 *x, *wqkv, *wo, *Are, *Aim, *Bre, *Bim, *Cre, *Cim, *logdt;
    char* ws;
};
__global__ __launch_bounds__(512) void k_prep(PrepArgs a) {
    char* __restrict__ ws = a.ws;
    __shared__ __attribute__((aligned(16))) char smem[8 * 648 * 4 + 2 * 648 * 4 + 8 * 1088 + 128 * 80 + 1024];
    constexpr int P_X = 10 * 648 * 4, P_W = P_X + 8 * 1088, P_CS = P_W + 128 * 80;
    const int tid = threadIdx.x, lane = tid & 63, w = tid >> 6;
    const int b = blockIdx.x;
    const int g = lane >> 4, l15 = lane & 15;
    PST(0);
    const int chl = w >> 2, kq4 = w & 3, ch = 2 * b + chl;
    u32x2 vAr[2], vAi[2], vBr[2], vBi[2], vCr[2], vCi[2];
#pragma unroll
    for (int ks = 0; ks < 2; ++ks) {
        const int n0 = ch * NM + 32 * kq4 + 16 * ks + 4 * g;
        vAr[ks] = *(const u32x2*)(a.Are + n0); vAi[ks] = *(const u32x2*)(a.Aim + n0);
        vBr[ks] = *(const u32x2*)(a.Bre + n0); vBi[ks] = *(const u32x2*)(a.Bim + n0);
        vCr[ks] = *(const u32x2*)(a.Cre + n0); vCi[ks] = *(const u32x2*)(a.Cim + n0);
    }
    const float ldt = bf2f(a.logdt[ch]);
    const u32x4 xrow = *(const u32x4*)(a.x + (size_t)(8 * b + w) * D + 8 * lane);
    const bool isq = b < 192;
    const int id2 = isq ? b : b - 192;
    const int kq = id2 & 3, nb = id2 >> 2;
    const u32x4 wv = *(const u32x4*)((isq ? a.wqkv : a.wo) + (size_t)(128 * kq + (tid >> 2)) * (isq ? 1536 : 512) + 32 * nb + 8 * (tid & 3));
    u32x4 cpf = {0u, 0u, 0u, 0u};
    {
        unsigned long long pc;
        int rel, len;
        asm volatile("s_getpc_b64 %0\n.Lpp_here:\n\ts_mov_b32 %1, .Lmk_begin-.Lpp_here\n\ts_mov_b32 %2, .Lmk_end-.Lmk_begin" : "=s"(pc), "=s"(rel), "=s"(len));
        const int off = ((b >> 3) * 512 + tid) * 16;
        if (off + 32 <= len) cpf = *(const u32x4*)(((pc + (long long)rel) & ~15ull) + off);
    }
    {
        u32x4 z = {0u, 0u, 0u, 0u};
        if (tid < 8) ((u32x4*)(ws + OFF_ACC))[b * 8 + tid] = z;
        const u32x4 pz = {POISON, POISON, POISON, POISON};
        ((u32x4*)(ws + OFF_XT1))[b * 512 + tid] = pz;
        ((u32x4*)(ws + OFF_XT2))[b * 512 + tid] = pz;
        ((u32x4*)(ws + OFF_KV))[b * 1024 + tid] = pz;
        ((u32x4*)(ws + OFF_KV))[b * 1024 + 512 + tid] = pz;
        ((u32x4*)(ws + OFF_OF))[b * 512 + tid] = pz;
    }
    {
        const float dt = rbf(expf(ldt));
        PST(1);
        const float lo = (float)(8 * l15);
        float hv[3];
#pragma unroll
        for (int mt = 0; mt < 3; ++mt) hv[mt] = hival(16 * mt + l15);
        f32x4 acc[3];
#pragma unroll
        for (int mt = 0; mt < 3; ++mt) acc[mt] = f32x4{0.f, 0.f, 0.f, 0.f};
#pragma unroll
        for (int ks = 0; ks < 2; ++ks) {
            u32x4 Bh, Bl, Ah[3], Al[3];
#pragma unroll
            for (int i = 0; i < 4; ++i) {
                const int wd = i >> 1;
                const bool hi = i & 1;
                float Ar = hi ? bfhi(vAr[ks][wd]) : bflo(vAr[ks][wd]), Ai = hi ? bfhi(vAi[ks][wd]) : bflo(vAi[ks][wd]);
                float Br = hi ? bfhi(vBr[ks][wd]) : bflo(vBr[ks][wd]), Bi = hi ? bfhi(vBi[ks][wd]) : bflo(vBi[ks][wd]);
                float Cr = hi ? bfhi(vCr[ks][wd]) : bflo(vCr[ks][wd]), Ci = hi ? bfhi(vCi[ks][wd]) : bflo(vCi[ks][wd]);
                const f2_t ef = {(dt * Ar) * 1.4426950408889634f, (dt * Ai) * 0.15915494309189535f};
                float dBr = dt * Br, dBi = dt * Bi;
                float cbr = Cr * dBr - Ci * dBi, cbi = Cr * dBi + Ci * dBr;
                f2_t e0 = cexp_l(ef, lo);
                f2_t gg = {cbr * e0[0] - cbi * e0[1], -(cbr * e0[1] + cbi * e0[0])};
                unsigned hh, ll;
                split2(gg, hh, ll);
                Bh[i] = hh; Bl[i] = ll;
#pragma unroll
                for (int mt = 0; mt < 3; ++mt) {
                    split2(cexp_l(ef, hv[mt]), hh, ll);
                    Ah[mt][i] = hh; Al[mt][i] = ll;
                }
            }
            bf16x8 bh = __builtin_bit_cast(bf16x8, Bh), bl = __builtin_bit_cast(bf16x8, Bl);
#pragma unroll
            for (int mt = 0; mt < 3; ++mt) {
                bf16x8 ah = __builtin_bit_cast(bf16x8, Ah[mt]), al = __builtin_bit_cast(bf16x8, Al[mt]);
                acc[mt] = MFMA16(al, bh, acc[mt]);
                acc[mt] = MFMA16(ah, bl, acc[mt]);
                acc[mt] = MFMA16(ah, bh, acc[mt]);
            }
        }
        PST(2);
        float* kw = (float*)smem + w * 648;
        const int p0 = 4 * (g & 1) + 128 * (g >> 1) + 8 * l15;
        *(f32x4*)(kw + p0) = acc[0];
        if (g < 2) *(f32x4*)(kw + 256 + 64 * g + 4 * l15) = acc[1];
        else {
            const int pb = 384 + 64 * (g - 2) + 2 * l15;
            *(f2_t*)(kw + pb) = f2_t{acc[1][0], acc[1][1]};
            *(f2_t*)(kw + pb + 32) = f2_t{acc[1][2], acc[1][3]};
        }
        if (g < 2) {
#pragma unroll
            for (int r = 0; r < 4; ++r) kw[512 + 16 * (4 * g + r) + l15] = acc[2][r];
        } else if (lane == 32) kw[640] = acc[2][0];
    }
    {
        *(u32x4*)(smem + P_X + w * 1088 + 16 * lane) = xrow;
        *(u32x4*)(smem + P_W + (tid >> 2) * 80 + 16 * (tid & 3)) = wv;
        float s = 0.f;
#pragma unroll
        for (int j = 0; j < 4; ++j) s = dot2(xrow[j], 0x3f803f80u, s);
        s = xrow16_sum(row16_sum(s));
        const float mean = s * (1.f / 512.f);
        float q = 0.f;
#pragma unroll
        for (int j = 0; j < 4; ++j) { float d0 = bflo(xrow[j]) - mean, d1 = bfhi(xrow[j]) - mean; q += d0 * d0 + d1 * d1; }
        q = xrow16_sum(row16_sum(q));
        if (lane == 0) ((f2_t*)(ws + OFF_ST0))[8 * b + w] = ln_scalars(mean, q * (1.f / 512.f));
    }
    asm volatile("" :: "v"(cpf));
    __syncthreads();
    PST(3);
    {
        const float* kq_ = (const float*)smem;
        float* kt = (float*)smem + 8 * 648;
        for (int i = tid; i < 2 * 648; i += 512) {
            const int c = i >= 648, pi = i - 648 * c;
            const float* kp = kq_ + c * (4 * 648) + pi;
            kt[i] = (kp[0] + kp[648]) + (kp[1296] + kp[1944]);
        }
        const int q = l15 >> 2, p = lane & 3;
        {
            const int d0 = 64 * w + 16 * g;
            const lds_char* src = (const lds_char*)smem + P_X + q * 1088 + (d0 + 4 * p) * 2;
            u32x2 r0 = __builtin_bit_cast(u32x2, TRREAD(src)), r1 = __builtin_bit_cast(u32x2, TRREAD(src + 4 * 1088));
            *(u32x4*)(ws + OFF_XT0 + (size_t)(d0 + l15) * (T * 2) + 16 * b) = u32x4{r0[0], r0[1], r1[0], r1[1]};
        }
        {
            const int nt2 = w & 1, k4 = w >> 1;
            const lds_char* src = (const lds_char*)smem + P_W + (32 * k4 + 8 * g + q) * 80 + (16 * nt2 + 4 * p) * 2;
            u32x2 r0 = __builtin_bit_cast(u32x2, TRREAD(src)), r1 = __builtin_bit_cast(u32x2, TRREAD(src + 4 * 80));
            u32x4 ov = {r0[0], r0[1], r1[0], r1[1]};
            const int n16 = 2 * nb + nt2, ks = 4 * kq + k4;
            size_t off;
            if (isq) off = OFF_WF + ((size_t)(((n16 & 31) * 3 + (n16 >> 5)) * 16 + ks) * 64 + lane) * 16;
            else off = OFF_WOF + ((size_t)(n16 * 16 + ks) * 64 + lane) * 16;
            *(u32x4*)(ws + off) = ov;
            float cs = 0.f;
#pragma unroll
            for (int j = 0; j < 4; ++j) cs = dot2(ov[j], 0x3f803f80u, cs);
            cs = xrow16_sum(cs);
            if (g == 0) ((float*)(smem + P_CS))[w * 16 + l15] = cs;
        }
        __syncthreads();
        if (isq && tid < 32) {
            const float* cp = (const float*)(smem + P_CS) + tid;
            ((float*)(ws + OFF_CS))[kq * 1536 + 32 * nb + tid] = (cp[0] + cp[32]) + (cp[64] + cp[96]);
        }
        for (int task = tid; task < 2 * 260; task += 512) {
            const int c = task >= 260, ci = task - 260 * c;
            const int m = 255 - ci;
            int base; unsigned offs;
            if (m < 32) { base = 8 * m; offs = 0x76543210u; }
            else if (m < 64) { base = 256 + 4 * (m - 32); offs = 0x43222100u; }
            else if (m < 128) { base = 384 + 2 * (m - 64); offs = 0x22111000u; }
            else { base = 512 + (m - 128); offs = 0x11100000u | ((unsigned)(m & 1) << 16); }
            if (m < 0) base = 0;
            float v[8];
#pragma unroll
            for (int j = 0; j < 8; ++j) {
                const int i = 7 - j;
                float val = kt[c * 648 + base + ((offs >> (4 * i)) & 15)];
                v[j] = m < 0 ? 0.f : val;
            }
            u32x4 o = {pk(v[0], v[1]), pk(v[2], v[3]), pk(v[4], v[5]), pk(v[6], v[7])};
            *(u32x4*)(ws + OFF_R + (size_t)(2 * b + c) * 4160 + 16 * ci) = o;
        }
    }
    PST(4);
}

constexpr int CV_RC = 4160, CV_RCH = 8 * CV_RC;
constexpr int CV_U0 = 2 * CV_RCH, CV_UPL = 1536, CV_UCH = 4 * CV_UPL;
constexpr int CV_Y0 = CV_U0 + 2 * CV_UCH;
constexpr int CV_YP = 64 * 144;
constexpr int Q_XCH = 81920;
constexpr int Q_PART = Q_XCH + 49152;
constexpr int A_ONES = Q_PART + 4096;
constexpr int PF_SCR = A_ONES + 20480;
constexpr int MEGA_LDS = PF_SCR + 8192;
static_assert(MEGA_LDS <= 163840 && CV_Y0 + 8 * CV_YP <= PF_SCR, "lds");

struct MegaArgs {
    const u16* Dskip;
    u16* out;
    char* ws;
};

constexpr unsigned SPIN_MAX = 2000000u;
DEV u32x4 sanit(u32x4 v) {
#pragma unroll
    for (int j = 0; j < 4; ++j) v[j] = min(v[j], 0xFFFFFFFEu);
    return v;
}
DEV bool has_poison(u32x4 v) { return max(max(v[0], v[1]), max(v[2], v[3])) == POISON; }
#define DMA_VALID(N, SRC, DST, LIVE) { \
    unsigned todo_ = (LIVE); \
    VM0(); \
    for (unsigned spins_ = 0; todo_ != 0u && spins_ < SPIN_MAX; ++spins_) { \
        if (spins_) __builtin_amdgcn_s_sleep(4); \
        _Pragma("unroll") for (int i = 0; i < (N); ++i) \
            if (todo_ & (1u << i)) __builtin_amdgcn_global_load_lds((const unsigned*)(SRC(i)), (lds_u32*)(DST(i)), 16, 0, SC1); \
        VM0(); \
        _Pragma("unroll") for (int i = 0; i < (N); ++i) \
            if (todo_ & (1u << i)) { \
                u32x4 v_ = *(const volatile u32x4*)((const char*)(DST(i)) + lane * 16); \
                if (!__any(has_poison(v_))) todo_ &= ~(1u << i); \
            } \
    } }
DEV float max3f(float a, float b, float c) { return __builtin_fmaxf(__builtin_fmaxf(a, b), c); }
DEV float max2f(float a, float b) { return __builtin_fmaxf(a, b); }
template <bool EDGE>
DEV u32x4 attn_body(const char* smem, int hw, int kh, int lane, int ab, const bf16x8 Qf, const bf16x8* wo, bf16x8 (&Wo)[16]) {
    const int r32 = lane & 31, hh = lane >> 5;
    const float NEG = -1.0e30f;
    const char* kb = smem + hw * 20480 + kh * 2048 + lane * 16;
    const char* vb = (r32 < 16 ? smem + hw * 20480 + kh * 2048 + 1024 : smem + A_ONES) + hh * 256 + (r32 & 15) * 16;
    bf16x8 Kf[9];
#pragma unroll
    for (int j = 0; j < 9; ++j) Kf[j] = *(const bf16x8*)(kb + j * 2048);
    f32x16 zero;
#pragma unroll
    for (int i = 0; i < 16; ++i) zero[i] = 0.f;
    float m0 = NEG, m1 = NEG;
#pragma unroll
    for (int j = 0; j < 9; ++j) {
        f32x16 S = MFMA32(Kf[j], Qf, zero);
        if (j == 0) {
#pragma unroll
            for (int i = 0; i < 16; ++i) if (!((i & 3) + 8 * (i >> 2) + 4 * hh > r32)) S[i] = NEG;
        }
        if (j == 8) {
#pragma unroll
            for (int i = 0; i < 16; ++i) if ((i & 3) + 8 * (i >> 2) + 4 * hh > r32) S[i] = NEG;
        }
        float a0 = m0, a1 = m1;
#pragma unroll
        for (int i = 0; i < 16; i += 4) { a0 = max3f(a0, S[i], S[i + 1]); a1 = max3f(a1, S[i + 2], S[i + 3]); }
        if (EDGE) { if (ab - 8 + j >= 0) { m0 = a0; m1 = a1; } }
        else { m0 = a0; m1 = a1; }
    }
    float m = max2f(m0, m1);
    {
        auto t = __builtin_amdgcn_permlane32_swap(__float_as_uint(m), __float_as_uint(m), false, false);
        m = max2f(__uint_as_float(t[0]), __uint_as_float(t[1]));
    }
    f32x16 nm;
#pragma unroll
    for (int i = 0; i < 16; ++i) nm[i] = -m;
    f32x16 O = zero;
#pragma unroll
    for (int j = 0; j < 9; ++j) {
        bf16x8 v0 = *(const bf16x8*)(vb + j * 2048), v1 = *(const bf16x8*)(vb + j * 2048 + 512);
        if (j < 8) { Wo[j] = wo[j * 64]; SB(); }
        f32x16 S = MFMA32(Kf[j], Qf, nm);
        if (j == 0) {
#pragma unroll
            for (int i = 0; i < 16; ++i) if (!((i & 3) + 8 * (i >> 2) + 4 * hh > r32)) S[i] = NEG;
        }
        if (j == 8) {
#pragma unroll
            for (int i = 0; i < 16; ++i) if ((i & 3) + 8 * (i >> 2) + 4 * hh > r32) S[i] = NEG;
        }
        u32x4 p0, p1;
#pragma unroll
        for (int i = 0; i < 4; ++i) {
            p0[i] = pk(__builtin_amdgcn_exp2f(S[2 * i]), __builtin_amdgcn_exp2f(S[2 * i + 1]));
            p1[i] = pk(__builtin_amdgcn_exp2f(S[8 + 2 * i]), __builtin_amdgcn_exp2f(S[8 + 2 * i + 1]));
        }
        if (!EDGE || ab - 8 + j >= 0) {
            O = MFMA32(v0, __builtin_bit_cast(bf16x8, p0), O);
            O = MFMA32(v1, __builtin_bit_cast(bf16x8, p1), O);
        }
    }
    const float inv = __builtin_amdgcn_rcpf(O[8]);
    const unsigned a0 = pk(O[0] * inv, O[1] * inv), a1 = pk(O[2] * inv, O[3] * inv);
    const unsigned b0 = pk(O[4] * inv, O[5] * inv), b1 = pk(O[6] * inv, O[7] * inv);
    auto sx = __builtin_amdgcn_permlane32_swap(a0, b0, false, false);
    auto sy = __builtin_amdgcn_permlane32_swap(a1, b1, false, false);
    return u32x4{sx[0], sy[0], sx[1], sy[1]};
}

template <int layer>
DEV void layer_body(const MegaArgs& a, char* smem) {
    char* ws = a.ws;
    const int tid0 = threadIdx.x;
    const int w = __builtin_amdgcn_readfirstlane(tid0 >> 6);
    const int b = blockIdx.x;
    const int tt = 4 * (b & 7) + ((b >> 3) & 3), hg = b >> 5;
    const int hw = w & 3, kh = w >> 2;
    const int h = 4 * hg + hw;
    const __amdgpu_buffer_rsrc_t rs = __builtin_amdgcn_make_buffer_rsrc((void*)ws, 0, WS_USED, 0x00020000);
    {
        const unsigned xin = layer == 0 ? OFF_XT0 : OFF_XT2;
        const unsigned xmid = layer == 0 ? OFF_XT1 : OFF_XT3;
        const unsigned kvb = OFF_KV + layer * 4 * MB;
        const unsigned ofb = OFF_OF + layer * 2 * MB;
        bf16x8 Wf[3][8];
        f32x4 csq = {0.f, 0.f, 0.f, 0.f}, csk = {0.f, 0.f, 0.f, 0.f};
        float csv = 0.f;
        {
            PHASE_IDS();
            const int c = tid >> 8, ti = tid & 255;
            u32x4 xv, sv[4], s0v[2], pf[8];
            if (layer == 0) {
                xv = *(const u32x4*)(ws + xin + (size_t)(2 * b + c) * (T * 2) + 16 * ti);
                s0v[0] = *(const u32x4*)(ws + OFF_ST0 + 32 * tid);
                s0v[1] = *(const u32x4*)(ws + OFF_ST0 + 32 * tid + 16);
            }
            const float dk = bf2f(a.Dskip[2 * b + c]);
            {
                const int ci2 = 256 + (tid & 3), c2 = (tid >> 2) & 1;
                const u32x4* src = (const u32x4*)(ws + OFF_R + (size_t)(2 * b + c) * 4160 + 16 * ti);
                const u32x4* src2 = (const u32x4*)(ws + OFF_R + (size_t)(2 * b + c2) * 4160 + 16 * (ci2 > 258 ? 258 : ci2));
                u32x4 v0 = src[0], v1 = src[1], z0 = src2[0], z1 = src2[1];
                if (layer == 0) {
                    const u32x4* pfs = (const u32x4*)(ws + OFF_WF + (size_t)(b >> 3) * 65536 + w * 8192 + lane * 16);
#pragma unroll
                    for (int i = 0; i < 8; ++i) pf[i] = pfs[i * 64];
                }
#pragma unroll
                for (int rep = 0; rep < 2; ++rep) {
                    if (rep == 1 && tid >= 8) break;
                    unsigned dw[8];
#pragma unroll
                    for (int j = 0; j < 4; ++j) { dw[j] = rep ? z0[j] : v0[j]; dw[4 + j] = rep ? z1[j] : v1[j]; }
                    char* dst = smem + (rep ? c2 * CV_RCH + 16 * (ci2 > 258 ? 258 : ci2) : c * CV_RCH + 16 * ti);
#pragma unroll
                    for (int s = 0; s < 8; ++s) {
                        u32x4 o;
#pragma unroll
                        for (int d = 0; d < 4; ++d) {
                            const int k = (s >> 1) + d;
                            o[d] = (s & 1) ? __builtin_amdgcn_alignbyte(dw[k + 1], dw[k], 2) : dw[k];
                        }
                        *(u32x4*)(dst + s * CV_RC) = o;
                    }
                }
                if (tid < 256) *(u32x4*)(smem + CV_U0 + (tid >> 5) * CV_UPL + 16 * (tid & 31)) = u32x4{0u, 0u, 0u, 0u};
            }
            STAMP(16 * layer + 1);
            f32x4* stl = (f32x4*)(smem + CV_Y0);
            if (layer != 0) {
                u32x4 av[4];
                VM0();
                bool bad = true;
                for (unsigned spins = 0; spins < SPIN_MAX; ++spins) {
                    asm volatile("" ::: "memory");
                    if (bad) {
                        xv = LD128(rs, xin + (unsigned)(2 * b + c) * (T * 2) + 16 * ti);
                        bad = has_poison(xv);
                    }
                    if (!__any(bad)) break;
                    __builtin_amdgcn_s_sleep(4);
                }
                BAR();
                bad = true;
                for (unsigned spins = 0; spins < SPIN_MAX; ++spins) {
                    asm volatile("" ::: "memory");
                    if (bad) {
#pragma unroll
                        for (int j = 0; j < 4; ++j) av[j] = LD128(rs, OFF_ACC + 8192 * j + 16 * tid);
                        bad = false;
#pragma unroll
                        for (int j = 0; j < 4; ++j) bad |= (av[j][1] >> 26) != 32u || (av[j][3] >> 26) != 32u;
                    }
                    if (!__any(bad)) break;
                    __builtin_amdgcn_s_sleep(8);
                }
                STAMP(16 + 2);
#pragma unroll
                for (int j = 0; j < 4; ++j) {
                    double s1 = (double)(int)((av[j][1] & 0x03FFFFFFu) - 0x01000000u) * 4294967296.0 + (double)av[j][0];
                    double s2 = (double)(int)((av[j][3] & 0x03FFFFFFu) - 0x01000000u) * 4294967296.0 + (double)av[j][2];
                    double mean = s1 * (1.0 / (1073741824.0 * 512.0));
                    double var = s2 * (1.0 / (16777216.0 * 512.0)) - mean * mean;
                    ((f2_t*)stl)[512 * j + tid] = ln_scalars((float)mean, fmaxf((float)var, 0.f));
                }
            } else {
                stl[2 * tid] = __builtin_bit_cast(f32x4, s0v[0]);
                stl[2 * tid + 1] = __builtin_bit_cast(f32x4, s0v[1]);
            }
            BAR();
#pragma unroll
            for (int j = 0; j < 4; ++j) sv[j] = __builtin_bit_cast(u32x4, stl[4 * ti + j]);
            u32x4 uv;
#pragma unroll
            for (int j = 0; j < 4; ++j) {
                float u0 = rbf(bflo(xv[j]) - __uint_as_float(sv[j][0])) * __uint_as_float(sv[j][1]);
                float u1 = rbf(bfhi(xv[j]) - __uint_as_float(sv[j][2])) * __uint_as_float(sv[j][3]);
                uv[j] = pk(u0, u1);
            }
            *(u32x4*)(smem + CV_U0 + c * CV_UCH + (ti & 3) * CV_UPL + 16 * (32 + (ti >> 2))) = uv;
            BAR();
            if (layer == 0) {
#pragma unroll
                for (int i = 0; i < 8; ++i) asm volatile("" :: "v"(pf[i]));
            }
            STAMP(16 * layer + 3);
            {
                const int cw = w >> 2, kk = w & 1, par = (w >> 1) & 1;
                const int r = lane & 31, hh = lane >> 5;
                const int rho = r >> 3, cc = r & 7;
                const char* pa = smem + cw * CV_RCH + (7 - cc) * CV_RC + 16 * (255 + 2 * kk - rho + hh) - 64 * par;
                const char* pb = smem + CV_U0 + cw * CV_UCH + (2 * kk + hh) * CV_UPL + 16 * (32 + r) - 16 * par;
                const bf16x8* wf = (const bf16x8*)(ws + OFF_WF) + (size_t)(h * 3) * 16 * 64 + (8 * kh) * 64 + lane;
                const float* cs = (const float*)(ws + OFF_CS);
                f32x4 cq[4], ck[4];
                float cv[4];
                f32x16 acc0, acc1;
#pragma unroll
                for (int i = 0; i < 16; ++i) { acc0[i] = 0.f; acc1[i] = 0.f; }
                bf16x8 a0 = *(const bf16x8*)(pa), a1 = *(const bf16x8*)(pa - 2048);
                bf16x8 b0 = *(const bf16x8*)(pb), b1 = *(const bf16x8*)(pb + 512);
#pragma unroll
                for (int it = 0; it < 16; ++it) {
                    bf16x8 na0, na1, nb0, nb1;
                    if (it < 15) {
                        na0 = *(const bf16x8*)(pa - 128 * (it + 1));
                        na1 = *(const bf16x8*)(pa - 128 * (it + 1) - 2048);
                        nb0 = *(const bf16x8*)(pb - 32 * (it + 1));
                        nb1 = *(const bf16x8*)(pb - 32 * (it + 1) + 512);
                    }
                    if (it < 14) {
                        const int id = it, i = id / 3, sct = id % 3;
                        Wf[sct][i] = wf[(sct * 16 + ((i + hw) & 7)) * 64];
                    }
                    if (it >= 12) {
                        const int kq = it - 12;
                        cq[kq] = *(const f32x4*)(cs + kq * 1536 + h * 16 + 4 * g);
                        ck[kq] = *(const f32x4*)(cs + kq * 1536 + 512 + h * 16 + 4 * g);
                        cv[kq] = cs[kq * 1536 + 1024 + h * 16 + l15];
                    }
                    SB();
                    acc0 = MFMA32(a0, b0, acc0);
                    acc1 = MFMA32(a0, b1, acc1);
                    acc1 = MFMA32(a1, b0, acc1);
                    SB();
                    if (it < 15) { a0 = na0; a1 = na1; b0 = nb0; b1 = nb1; }
                }
                char* yp = smem + CV_Y0 + w * CV_YP + r * 144 + 16 * hh;
#pragma unroll
                for (int q = 0; q < 4; ++q) {
                    f32x4 v0 = {acc0[4 * q], acc0[4 * q + 1], acc0[4 * q + 2], acc0[4 * q + 3]};
                    f32x4 v1 = {acc1[4 * q], acc1[4 * q + 1], acc1[4 * q + 2], acc1[4 * q + 3]};
                    *(f32x4*)(yp + 32 * q) = v0;
                    *(f32x4*)(yp + 32 * 144 + 32 * q) = v1;
                }
                SB();
                csq = (cq[0] + cq[1]) + (cq[2] + cq[3]);
                csk = (ck[0] + ck[1]) + (ck[2] + ck[3]);
                csv = (cv[0] + cv[1]) + (cv[2] + cv[3]);
            }
            STAMP(16 * layer + 4);
            BAR();
            {
                const char* y0 = smem + CV_Y0 + (4 * c) * CV_YP + (ti >> 2) * 144 + (ti & 3) * 32;
                f32x4 pp[8];
#pragma unroll
                for (int p = 0; p < 4; ++p) { pp[2 * p] = *(const f32x4*)(y0 + p * CV_YP); pp[2 * p + 1] = *(const f32x4*)(y0 + p * CV_YP + 16); }
                f32x4 ya = (pp[0] + pp[2]) + (pp[4] + pp[6]), yb = (pp[1] + pp[3]) + (pp[5] + pp[7]);
                float y[8] = {ya[0], ya[1], ya[2], ya[3], yb[0], yb[1], yb[2], yb[3]};
                u32x4 o;
#pragma unroll
                for (int j = 0; j < 4; ++j) {
                    float s0 = rbf(rbf(y[2 * j]) + rbf(bflo(uv[j]) * dk));
                    float s1 = rbf(rbf(y[2 * j + 1]) + rbf(bfhi(uv[j]) * dk));
                    o[j] = pk(bflo(xv[j]) + s0, bfhi(xv[j]) + s1);
                }
                ST128(sanit(o), rs, xmid + (unsigned)(2 * b + c) * (T * 2) + 16 * ti);
                SB();
                const bf16x8* wf = (const bf16x8*)(ws + OFF_WF) + (size_t)(h * 3) * 16 * 64 + (8 * kh) * 64 + lane;
#pragma unroll
                for (int id = 14; id < 24; ++id) Wf[id % 3][id / 3] = wf[((id % 3) * 16 + ((id / 3 + hw) & 7)) * 64];
            }
            BAR();
            STAMP(16 * layer + 5);
        }
        u32x2 xres[2];
        bf16x8 Qf;
        {
            PHASE_IDS();
            STAMP(16 * layer + 6);
            {
                const int rr = lane >> 3, chp = lane & 7;
                const int f = ((rr >> 1) & 1) | ((w & 1) << 1);
                const char* src0 = ws + xmid + (size_t)(8 * w + rr) * (T * 2) + 128 * tt + 16 * (chp ^ (2 * f));
#define XSRC(i) (src0 + (size_t)(i) * (64 * T * 2))
#define XDST(i) (smem + 1024 * (w + 8 * (i)))
                DMA_VALID(8, XSRC, XDST, 0xFFu);
            }
            BAR();
            STAMP(16 * layer + 7);
            if (layer == 0) {
                const u32x4 pz = {POISON, POISON, POISON, POISON};
                ST128(pz, rs, OFF_XT3 + (unsigned)(b * 512 + tid) * 16);
                ST128(pz, rs, OFF_KV + 4 * MB + (unsigned)(b * 1024 + tid) * 16);
                ST128(pz, rs, OFF_KV + 4 * MB + (unsigned)(b * 1024 + 512 + tid) * 16);
                ST128(pz, rs, OFF_OF + 2 * MB + (unsigned)(b * 512 + tid) * 16);
            }
            {
                const int d = 64 * hg + 16 * hw + l15;
                const int f = ((d >> 1) & 1) | (((d >> 3) & 1) << 1);
#pragma unroll
                for (int i = 0; i < 2; ++i)
                    xres[i] = *(const u32x2*)(smem + d * 128 + 16 * ((4 * kh + 2 * i + (g >> 1)) ^ (2 * f)) + 8 * (g & 1));
            }
            f32x4 acc[3][4];
#pragma unroll
            for (int s = 0; s < 3; ++s)
#pragma unroll
                for (int i = 0; i < 4; ++i) acc[s][i] = f32x4{0.f, 0.f, 0.f, 0.f};
            float s1[4] = {0.f, 0.f, 0.f, 0.f}, s2[4] = {0.f, 0.f, 0.f, 0.f};
            const int q = l15 >> 2, p = lane & 3;
            const int f = ((q >> 1) & 1) | ((g & 1) << 1);
            const lds_char* lbase = (const lds_char*)smem + kh * 32768 + (8 * g + q) * 128 + 8 * (p & 1);
            bf16x8 xf[4], xn[4];
#define TRLD(dst, i_) { const lds_char* lb_ = lbase + (((i_) + hw) & 7) * 4096; _Pragma("unroll") for (int ti = 0; ti < 4; ++ti) { \
                const int ch = (2 * (ti ^ (2 * kh)) + (p >> 1)) ^ (2 * f); \
                v4s x0 = TRREAD(lb_ + 16 * ch); \
                v4s x1 = TRREAD(lb_ + 512 + 16 * ch); \
                dst[ti] = bf16x8{x0[0], x0[1], x0[2], x0[3], x1[0], x1[1], x1[2], x1[3]}; } }
            TRLD(xf, 0);
#pragma unroll
            for (int i = 0; i < 8; ++i) {
                if (i < 7) { TRLD(xn, i + 1); }
                SB();
#pragma unroll
                for (int ti = 0; ti < 4; ++ti) {
                    acc[0][ti] = MFMA16(Wf[0][i], xf[ti], acc[0][ti]);
                    acc[1][ti] = MFMA16(Wf[1][i], xf[ti], acc[1][ti]);
                    acc[2][ti] = MFMA16(xf[ti], Wf[2][i], acc[2][ti]);
                    if ((i & 3) == 0) {
                        u32x4 xu = __builtin_bit_cast(u32x4, xf[ti]);
#pragma unroll
                        for (int j = 0; j < 4; ++j) { s1[ti] = dot2(xu[j], 0x3f803f80u, s1[ti]); s2[ti] = dot2(xu[j], xu[j], s2[ti]); }
                        asm volatile("" : "+v"(s1[ti]), "+v"(s2[ti]));
                    }
                }
                SB();
                if (i < 7) {
#pragma unroll
                    for (int ti = 0; ti < 4; ++ti) xf[ti] = xn[ti];
                }
            }
            f2_t* part = (f2_t*)(smem + Q_PART);
#pragma unroll
            for (int ti = 0; ti < 4; ++ti) {
                float a1 = xrow16_sum(s1[ti]), a2 = xrow16_sum(s2[ti]);
                if (g == ti) part[w * 64 + 16 * (ti ^ (2 * kh)) + l15] = f2_t{a1, a2};
            }
            {
                char* xp = smem + Q_XCH + w * 6144 + lane * 16;
#pragma unroll
                for (int s = 0; s < 3; ++s)
#pragma unroll
                    for (int i = 0; i < 2; ++i) *(f32x4*)(xp + (s * 2 + i) * 1024) = acc[s][2 + i];
            }
            BAR();
            STAMP(16 * layer + 8);
            {
                f2_t st;
                {
                    float x1 = 0.f, x2 = 0.f;
#pragma unroll
                    for (int i = 0; i < 8; ++i) { f2_t v = part[i * 64 + 32 * kh + (lane & 31)]; x1 += v[0]; x2 += v[1]; }
                    float mean = x1 * (1.f / 512.f);
                    float var = x2 * (1.f / 512.f) - mean * mean;
                    st = ln_scalars(mean, fmaxf(var, 0.f));
                }
                const char* xp = smem + Q_XCH + (w ^ 4) * 6144 + lane * 16;
                const float qs = 0.25f * 1.4426950408889634f;
                u32x4 qo, ko, vo;
#pragma unroll
                for (int i = 0; i < 2; ++i) {
                    f32x4 aq = *(const f32x4*)(xp + (0 + i) * 1024), ak = *(const f32x4*)(xp + (2 + i) * 1024), av = *(const f32x4*)(xp + (4 + i) * 1024);
                    aq += acc[0][i];
                    ak += acc[1][i];
                    av += acc[2][i];
                    const float m0 = __shfl(st[0], 16 * i + l15), r0 = __shfl(st[1], 16 * i + l15);
                    float qv[4], kv[4], vv[4];
#pragma unroll
                    for (int r = 0; r < 4; ++r) {
                        qv[r] = rbf((aq[r] - m0 * csq[r]) * r0) * qs;
                        kv[r] = (ak[r] - m0 * csk[r]) * r0;
                        const float m1 = __shfl(st[0], 16 * i + 4 * g + r), r1 = __shfl(st[1], 16 * i + 4 * g + r);
                        vv[r] = (av[r] - m1 * csv) * r1;
                    }
                    qo[2 * i] = pk(qv[0], qv[1]); qo[2 * i + 1] = pk(qv[2], qv[3]);
                    ko[2 * i] = pk(kv[0], kv[1]); ko[2 * i + 1] = pk(kv[2], kv[3]);
                    vo[2 * i] = pk(vv[0], vv[1]); vo[2 * i + 1] = pk(vv[2], vv[3]);
                }
                {
                    auto qx = __builtin_amdgcn_permlane16_swap(qo[0], qo[2], false, false);
                    auto qy = __builtin_amdgcn_permlane16_swap(qo[1], qo[3], false, false);
                    Qf = __builtin_bit_cast(bf16x8, (u32x4{qx[0], qy[0], qx[1], qy[1]}));
                    auto kx = __builtin_amdgcn_permlane16_swap(ko[0], ko[2], false, false);
                    auto ky = __builtin_amdgcn_permlane16_swap(ko[1], ko[3], false, false);
                    ko = u32x4{kx[0], ky[0], kx[1], ky[1]};
                    auto vx = __builtin_amdgcn_permlane32_swap(vo[0], vo[2], false, false);
                    auto vy = __builtin_amdgcn_permlane32_swap(vo[1], vo[3], false, false);
                    vo = u32x4{vx[0], vy[0], vx[1], vy[1]};
                }
                const unsigned go = kvb + (unsigned)(h * 64 + 2 * tt + kh) * 2048 + lane * 16;
                ko = sanit(ko); vo = sanit(vo);
                ST128(ko, rs, go);
                ST128(vo, rs, go + 1024);
                char* lo = smem + hw * 20480 + (8 + kh) * 2048 + lane * 16;
                *(u32x4*)(lo) = ko;
                *(u32x4*)(lo + 1024) = vo;
                const u32x4 on = {0x3f803f80u, 0x3f803f80u, 0x3f803f80u, 0x3f803f80u};
                *(u32x4*)(smem + A_ONES + tid * 16) = on;
                *(u32x4*)(smem + A_ONES + 8192 + tid * 16) = on;
                if (tid < 256) *(u32x4*)(smem + A_ONES + 16384 + tid * 16) = on;
            }
            STAMP(16 * layer + 10);
        }
        bf16x8 Wo[16];
        {
            PHASE_IDS();
            {
                const int P0 = 2 * tt - 8 + 4 * kh;
                const char* src0 = ws + kvb + (size_t)(h * 64 + P0) * 2048 + lane * 16;
                char* dst0 = smem + hw * 20480 + 4 * kh * 2048;
                const unsigned live = P0 >= 0 ? 0xFFu : (P0 <= -4 ? 0u : (0xFFu << (2 * (-P0))) & 0xFFu);
#define KSRC(i) (src0 + (i) * 1024)
#define KDST(i) (dst0 + (i) * 1024)
                DMA_VALID(8, KSRC, KDST, live);
            }
            BAR();
            STAMP(16 * layer + 12);
            const bf16x8* wo = (const bf16x8*)(ws + OFF_WOF) + (size_t)((4 * hg + hw) * 16) * 64 + lane;
            const int ab = 2 * tt + kh;
            u32x4 o = ab < 8 ? attn_body<true>(smem, hw, kh, lane, ab, Qf, wo, Wo) : attn_body<false>(smem, hw, kh, lane, ab, Qf, wo, Wo);
            {
                const int tile = 2 * ab + (g & 1);
                const int gp = 2 * (h & 1) + (g >> 1);
                ST128(sanit(o), rs, ofb + (unsigned)((tile * 16 + (h >> 1)) * 64 + l15 + 16 * gp) * 16);
                SB();
#pragma unroll
                for (int ks = 8; ks < 16; ++ks) Wo[ks] = wo[ks * 64];
            }
            STAMP(16 * layer + 9);
            BAR();
            STAMP(16 * layer + 13);
        }
        {
            PHASE_IDS();
            STAMP(16 * layer + 14);
            {
                const char* src0 = ws + ofb + (size_t)(4 * tt * 16 + w) * 1024 + lane * 16;
#define OSRC(i) (src0 + (i) * 8192)
                DMA_VALID(8, OSRC, XDST, 0xFFu);
            }
            BAR();
            STAMP(16 * layer + 11);
            f32x4 acc[2][2];
#pragma unroll
            for (int i = 0; i < 2; ++i) { acc[i][0] = f32x4{0.f, 0.f, 0.f, 0.f}; acc[i][1] = f32x4{0.f, 0.f, 0.f, 0.f}; }
            {
                const char* op = smem + (2 * kh) * 16384 + lane * 16;
                bf16x8 of[2][8], og[2][8];
#pragma unroll
                for (int ks = 0; ks < 8; ++ks)
#pragma unroll
                    for (int i = 0; i < 2; ++i) of[i][ks] = *(const bf16x8*)(op + (i * 16 + ks) * 1024);
                SB();
#pragma unroll
                for (int ks = 0; ks < 8; ++ks) {
#pragma unroll
                    for (int i = 0; i < 2; ++i) og[i][ks] = *(const bf16x8*)(op + (i * 16 + 8 + ks) * 1024);
#pragma unroll
                    for (int i = 0; i < 2; ++i) acc[i][ks & 1] = MFMA16(of[i][ks], Wo[ks], acc[i][ks & 1]);
                    SB();
                }
#pragma unroll
                for (int ks = 0; ks < 8; ++ks)
#pragma unroll
                    for (int i = 0; i < 2; ++i) acc[i][ks & 1] = MFMA16(og[i][ks], Wo[8 + ks], acc[i][ks & 1]);
            }
            u32x2 o[2];
#pragma unroll
            for (int i = 0; i < 2; ++i) {
                f32x4 v = acc[i][0] + acc[i][1];
                o[i][0] = pk(bflo(xres[i][0]) + rbf(v[0]), bfhi(xres[i][0]) + rbf(v[1]));
                o[i][1] = pk(bflo(xres[i][1]) + rbf(v[2]), bfhi(xres[i][1]) + rbf(v[3]));
            }
            if (layer == 0) {
                float sel = 0.f;
#pragma unroll
                for (int i = 0; i < 2; ++i)
#pragma unroll
                    for (int r = 0; r < 4; ++r) {
                        const float x = (r & 1) ? bfhi(o[i][r >> 1]) : bflo(o[i][r >> 1]);
                        const float a1 = row16_sum(x), a2 = row16_sum(x * x);
                        if (l15 == 2 * (4 * i + r)) sel = a1;
                        if (l15 == 2 * (4 * i + r) + 1) sel = a2;
                    }
                {
                    const int e = l15 >> 1;
                    const int t = 64 * tt + 32 * kh + 16 * (e >> 2) + 4 * g + (e & 3);
                    const float sc = (l15 & 1) ? 16777216.0f : 1073741824.0f;
                    long long iv = (long long)__builtin_rintf(fminf(fmaxf(sel * sc, -1.0e15f), 1.0e15f)) + (1ll << 51) + (1ll << 58);
                    atomicAdd((unsigned long long*)(ws + OFF_ACC) + (size_t)t * 2 + (l15 & 1), (unsigned long long)iv);
                }
                SB();
                {
                    auto sx = __builtin_amdgcn_permlane16_swap(o[0][0], o[1][0], false, false);
                    auto sy = __builtin_amdgcn_permlane16_swap(o[0][1], o[1][1], false, false);
                    u32x4 ov = {sx[0], sy[0], sx[1], sy[1]};
                    ST128(sanit(ov), rs, OFF_XT2 + (unsigned)(64 * hg + 16 * hw + l15) * (T * 2) + 128 * tt + 64 * kh + 32 * (g & 1) + 16 * (g >> 1));
                }
                BAR();
            } else {
                BAR();
                u16* img = (u16*)smem;
#pragma unroll
                for (int i = 0; i < 2; ++i)
#pragma unroll
                    for (int r = 0; r < 4; ++r)
                        img[(32 * kh + 16 * i + 4 * g + r) * 72 + 16 * hw + l15] = (u16)((r & 1) ? (o[i][r >> 1] >> 16) : (o[i][r >> 1] & 0xffff));
                BAR();
                const int t = tid >> 3, nc = tid & 7;
                u32x4 r = *(const u32x4*)(img + t * 72 + 8 * nc);
                *(u32x4*)(a.out + (size_t)(64 * tt + t) * D + 64 * hg + 8 * nc) = r;
            }
            STAMP(16 * layer + 15);
        }
    }
}

__global__ __launch_bounds__(512, 2) void k_mega(MegaArgs a) {
    extern __shared__ __attribute__((aligned(16))) char smem[];
    char* ws = a.ws;
    asm volatile(".Lmk_begin:");
    STAMP(0);
    layer_body<0>(a, smem);
    layer_body<1>(a, smem);
    asm volatile(".Lmk_end:");
}

extern "C" void kernel_launch(void* const* d_in, const int* in_sizes, int n_in, void* d_out, int out_size, void* d_ws,
                              size_t ws_size, hipStream_t stream) {
    char* ws = (char*)d_ws;
    PrepArgs pa;
    pa.x = (const u16*)d_in[0];
    pa.wqkv = (const u16*)d_in[10];
    pa.wo = (const u16*)d_in[11];
    pa.Are = (const u16*)d_in[1];
    pa.Aim = (const u16*)d_in[2];
    pa.Bre = (const u16*)d_in[3];
    pa.Bim = (const u16*)d_in[4];
    pa.Cre = (const u16*)d_in[5];
    pa.Cim = (const u16*)d_in[6];
    pa.logdt = (const u16*)d_in[7];
    pa.ws = ws;
    MegaArgs ma;
    ma.Dskip = (const u16*)d_in[8];
    ma.out = (u16*)d_out;
    ma.ws = ws;
    (void)hipFuncSetAttribute((const void*)k_mega, hipFuncAttributeMaxDynamicSharedMemorySize, MEGA_LDS);
    k_prep<<<256, 512, 0, stream>>>(pa);
    k_mega<<<256, 512, MEGA_LDS, stream>>>(ma);
}
```

```cpp
#include <hip/hip_runtime.h>
#include <hip/hip_bf16.h>
#include <stdint.h>

typedef short bf16x8 __attribute__((ext_vector_type(8)));
typedef short bf16x4 __attribute__((ext_vector_type(4)));
typedef float f32x4 __attribute__((ext_vector_type(4)));
typedef float f32x16 __attribute__((ext_vector_type(16)));
typedef unsigned u32x4 __attribute__((ext_vector_type(4)));
typedef unsigned u32x2 __attribute__((ext_vector_type(2)));
typedef __bf16 bf2_t __attribute__((ext_vector_type(2)));
typedef float f2_t __attribute__((ext_vector_type(2)));
typedef unsigned short u16;
typedef __attribute__((address_space(3))) char lds_char;
typedef __attribute__((address_space(3))) unsigned lds_u32;

#define DEV __device__ __forceinline__
#define STAMP(i) do {} while (0)

#define PST(i) do {} while (0)
constexpr int T = 2048, D = 512, NM = 128;

constexpr unsigned MB = 1024 * 1024;
constexpr unsigned OFF_XT0 = 0, OFF_XT1 = 2 * MB, OFF_XT2 = 4 * MB, OFF_XT3 = 6 * MB;
constexpr unsigned OFF_WF = 8 * MB;
constexpr unsigned OFF_WOF = 9 * MB + 512 * 1024;
constexpr unsigned OFF_CS = 10 * MB;
constexpr unsigned OFF_ST0 = 10 * MB + 64 * 1024;
constexpr unsigned OFF_ACC = 10 * MB + 128 * 1024;
constexpr unsigned OFF_FLAGS = 10 * MB + 192 * 1024;
constexpr unsigned OFF_KV = 12 * MB;
constexpr unsigned OFF_OF = 20 * MB;
constexpr unsigned OFF_R = 24 * MB;
constexpr unsigned WS_USED = 28 * MB;

DEV float bf2f(unsigned v) { return __uint_as_float(v << 16); }
DEV float bflo(unsigned v) { return __uint_as_float(v << 16); }
DEV float bfhi(unsigned v) { return __uint_as_float(v & 0xffff0000u); }
DEV unsigned pk(float lo, float hi) {
    f2_t v = {lo, hi};
    return __builtin_bit_cast(unsigned, __builtin_convertvector(v, bf2_t));
}
DEV float rbf(float x) { return __uint_as_float(pk(x, 0.f) << 16); }
DEV float dot2(unsigned a, unsigned b, float acc) {
    return __builtin_amdgcn_fdot2_f32_bf16(__builtin_bit_cast(bf2_t, a), __builtin_bit_cast(bf2_t, b), acc, false);
}
DEV float dpp_ror8(float x) { return __builtin_bit_cast(float, __builtin_amdgcn_mov_dpp(__builtin_bit_cast(int, x), 0x128, 0xf, 0xf, true)); }
DEV float xrow16_max(float x) {
    auto s = __builtin_amdgcn_permlane16_swap(__float_as_uint(x), __float_as_uint(x), false, false);
    x = fmaxf(__uint_as_float(s[0]), __uint_as_float(s[1]));
    auto t = __builtin_amdgcn_permlane32_swap(__float_as_uint(x), __float_as_uint(x), false, false);
    return fmaxf(__uint_as_float(t[0]), __uint_as_float(t[1]));
}
DEV float xrow16_sum(float x) {
    auto s = __builtin_amdgcn_permlane16_swap(__float_as_uint(x), __float_as_uint(x), false, false);
    x = __uint_as_float(s[0]) + __uint_as_float(s[1]);
    auto t = __builtin_amdgcn_permlane32_swap(__float_as_uint(x), __float_as_uint(x), false, false);
    return __uint_as_float(t[0]) + __uint_as_float(t[1]);
}

#define MFMA16(a, b, c) __builtin_amdgcn_mfma_f32_16x16x32_bf16(a, b, c, 0, 0, 0)
#define MFMA32(a, b, c) __builtin_amdgcn_mfma_f32_32x32x16_bf16(a, b, c, 0, 0, 0)
#define MFMA16K16(a, b, c) __builtin_amdgcn_mfma_f32_16x16x16bf16_1k(a, b, c, 0, 0, 0)
#define SC1 16
#define LD128(rs, off) __builtin_amdgcn_raw_buffer_load_b128(rs, off, 0, SC1)
#define ST128(v, rs, off) __builtin_amdgcn_raw_buffer_store_b128(v, rs, off, 0, SC1)
#define VM0() asm volatile("s_waitcnt vmcnt(0)" ::: "memory")
typedef short v4s __attribute__((ext_vector_type(4)));
#define TRREAD(p) __builtin_amdgcn_ds_read_tr16_b64_v4i16((__attribute__((address_space(3))) v4s*)(p))
#define SB() __builtin_amdgcn_sched_barrier(0)
#define BAR() asm volatile("s_waitcnt lgkmcnt(0)\n\ts_barrier" ::: "memory")
#define PHASE_IDS() int tid = tid0; asm volatile("" : "+v"(tid)); const int lane = tid & 63, g = lane >> 4, l15 = lane & 15; (void)g; (void)l15;

DEV f2_t cexp_l(f2_t ef, float l) {
    f2_t t = ef * l;
    float e = __builtin_amdgcn_exp2f(t[0]);
    float fr = __builtin_amdgcn_fractf(t[1]);
    f2_t cs = {__builtin_amdgcn_cosf(fr), __builtin_amdgcn_sinf(fr)};
    return cs * e;
}
DEV void split2(f2_t v, unsigned& hi, unsigned& lo) {
    hi = pk(v[0], v[1]);
    f2_t hf = {bflo(hi), bfhi(hi)};
    f2_t r = v - hf;
    lo = pk(r[0], r[1]);
}
DEV float hival(int row) {
    int v;
    if (row < 16) v = (row & 7) + 128 * (row >> 3);
    else if (row < 24) { int i = row - 16; v = 256 + 2 * (i & 3) + 128 * (i >> 2); }
    else if (row < 32) { int i = row - 24; v = 512 + 4 * (i & 1) + 128 * (i >> 1); }
    else if (row < 40) v = 1024 + 128 * (row - 32);
    else if (row == 40) v = 2048;
    else v = 0;
    return (float)v;
}
DEV f2_t ln_scalars(float mean, float var) {
    float mb = rbf(mean);
    float vb = rbf(var);
    float sb = rbf(__builtin_amdgcn_sqrtf(rbf(vb + 1e-5f)));
    f2_t r = {mb, __builtin_amdgcn_rcpf(sb)};
    return r;
}

template <int CTRL> DEV float dppf(float x) { return __builtin_bit_cast(float, __builtin_amdgcn_mov_dpp(__builtin_bit_cast(int, x), CTRL, 0xf, 0xf, true)); }
DEV float row16_sum(float x) {
    x += dppf<0xB1>(x);
    x += dppf<0x4E>(x);
    x += dppf<0x124>(x);
    x += dppf<0x128>(x);
    return x;
}

constexpr unsigned POISON = 0xFFFFFFFFu;
struct PrepArgs {
    const u16 *x, *wqkv, *wo, *Are, *Aim, *Bre, *Bim, *Cre, *Cim, *logdt;
    char* ws;
};
__global__ __launch_bounds__(512) void k_prep(PrepArgs a) {
    char* __restrict__ ws = a.ws;
    __shared__ __attribute__((aligned(16))) char smem[8 * 648 * 4 + 2 * 648 * 4 + 8 * 1088 + 128 * 80 + 1024];
    constexpr int P_X = 10 * 648 * 4, P_W = P_X + 8 * 1088, P_CS = P_W + 128 * 80;
    const int tid = threadIdx.x, lane = tid & 63, w = tid >> 6;
    const int b = blockIdx.x;
    const int g = lane >> 4, l15 = lane & 15;
    PST(0);
    const int chl = w >> 2, kq4 = w & 3, ch = 2 * b + chl;
    u32x2 vAr[2], vAi[2], vBr[2], vBi[2], vCr[2], vCi[2];
#pragma unroll
    for (int ks = 0; ks < 2; ++ks) {
        const int n0 = ch * NM + 32 * kq4 + 16 * ks + 4 * g;
        vAr[ks] = *(const u32x2*)(a.Are + n0); vAi[ks] = *(const u32x2*)(a.Aim + n0);
        vBr[ks] = *(const u32x2*)(a.Bre + n0); vBi[ks] = *(const u32x2*)(a.Bim + n0);
        vCr[ks] = *(const u32x2*)(a.Cre + n0); vCi[ks] = *(const u32x2*)(a.Cim + n0);
    }
    const float ldt = bf2f(a.logdt[ch]);
    const u32x4 xrow = *(const u32x4*)(a.x + (size_t)(8 * b + w) * D + 8 * lane);
    const bool isq = b < 192;
    const int id2 = isq ? b : b - 192;
    const int kq = id2 & 3, nb = id2 >> 2;
    const u32x4 wv = *(const u32x4*)((isq ? a.wqkv : a.wo) + (size_t)(128 * kq + (tid >> 2)) * (isq ? 1536 : 512) + 32 * nb + 8 * (tid & 3));
    {
        u32x4 z = {0u, 0u, 0u, 0u};
        if (tid < 8) ((u32x4*)(ws + OFF_ACC))[b * 8 + tid] = z;
        const __amdgpu_buffer_rsrc_t rsp = __builtin_amdgcn_make_buffer_rsrc((void*)ws, 0, WS_USED, 0x00020000);
        const u32x4 pz = {POISON, POISON, POISON, POISON};
        ST128(pz, rsp, OFF_XT1 + (unsigned)(b * 512 + tid) * 16u);
        ST128(pz, rsp, OFF_XT2 + (unsigned)(b * 512 + tid) * 16u);
        ST128(pz, rsp, OFF_KV + (unsigned)(b * 1024 + tid) * 16u);
        ST128(pz, rsp, OFF_KV + (unsigned)(b * 1024 + 512 + tid) * 16u);
        ST128(pz, rsp, OFF_OF + (unsigned)(b * 512 + tid) * 16u);
    }
    {
        const float dt = rbf(expf(ldt));
        PST(1);
        const float lo = (float)(8 * l15);
        float hv[3];
#pragma unroll
        for (int mt = 0; mt < 3; ++mt) hv[mt] = hival(16 * mt + l15);
        f32x4 acc[3];
#pragma unroll
        for (int mt = 0; mt < 3; ++mt) acc[mt] = f32x4{0.f, 0.f, 0.f, 0.f};
#pragma unroll
        for (int ks = 0; ks < 2; ++ks) {
            u32x4 Bh, Bl, Ah[3];
#pragma unroll
            for (int i = 0; i < 4; ++i) {
                const int wd = i >> 1;
                const bool hi = i & 1;
                float Ar = hi ? bfhi(vAr[ks][wd]) : bflo(vAr[ks][wd]), Ai = hi ? bfhi(vAi[ks][wd]) : bflo(vAi[ks][wd]);
                float Br = hi ? bfhi(vBr[ks][wd]) : bflo(vBr[ks][wd]), Bi = hi ? bfhi(vBi[ks][wd]) : bflo(vBi[ks][wd]);
                float Cr = hi ? bfhi(vCr[ks][wd]) : bflo(vCr[ks][wd]), Ci = hi ? bfhi(vCi[ks][wd]) : bflo(vCi[ks][wd]);
                const f2_t ef = {(dt * Ar) * 1.4426950408889634f, (dt * Ai) * 0.15915494309189535f};
                float dBr = dt * Br, dBi = dt * Bi;
                float cbr = Cr * dBr - Ci * dBi, cbi = Cr * dBi + Ci * dBr;
                f2_t e0 = cexp_l(ef, lo);
                f2_t gg = {cbr * e0[0] - cbi * e0[1], -(cbr * e0[1] + cbi * e0[0])};
                unsigned hh, ll;
                split2(gg, hh, ll);
                Bh[i] = hh; Bl[i] = ll;
#pragma unroll
                for (int mt = 0; mt < 3; ++mt) {
                    const f2_t ea = cexp_l(ef, hv[mt]);
                    Ah[mt][i] = pk(ea[0], ea[1]);
                }
            }
            bf16x8 bh = __builtin_bit_cast(bf16x8, Bh), bl = __builtin_bit_cast(bf16x8, Bl);
#pragma unroll
            for (int mt = 0; mt < 3; ++mt) {
                bf16x8 ah = __builtin_bit_cast(bf16x8, Ah[mt]);
                acc[mt] = MFMA16(ah, bl, acc[mt]);
                acc[mt] = MFMA16(ah, bh, acc[mt]);
            }
        }
        PST(2);
        float* kw = (float*)smem + w * 648;
        const int p0 = 4 * (g & 1) + 128 * (g >> 1) + 8 * l15;
        *(f32x4*)(kw + p0) = acc[0];
        if (g < 2) *(f32x4*)(kw + 256 + 64 * g + 4 * l15) = acc[1];
        else {
            const int pb = 384 + 64 * (g - 2) + 2 * l15;
            *(f2_t*)(kw + pb) = f2_t{acc[1][0], acc[1][1]};
            *(f2_t*)(kw + pb + 32) = f2_t{acc[1][2], acc[1][3]};
        }
        if (g < 2) {
#pragma unroll
            for (int r = 0; r < 4; ++r) kw[512 + 16 * (4 * g + r) + l15] = acc[2][r];
        } else if (lane == 32) kw[640] = acc[2][0];
    }
    {
        *(u32x4*)(smem + P_X + w * 1088 + 16 * lane) = xrow;
        *(u32x4*)(smem + P_W + (tid >> 2) * 80 + 16 * (tid & 3)) = wv;
        float s = 0.f;
#pragma unroll
        for (int j = 0; j < 4; ++j) s = dot2(xrow[j], 0x3f803f80u, s);
        s = xrow16_sum(row16_sum(s));
        const float mean = s * (1.f / 512.f);
        float q = 0.f;
#pragma unroll
        for (int j = 0; j < 4; ++j) { float d0 = bflo(xrow[j]) - mean, d1 = bfhi(xrow[j]) - mean; q += d0 * d0 + d1 * d1; }
        q = xrow16_sum(row16_sum(q));
        if (lane == 0) ((f2_t*)(ws + OFF_ST0))[8 * b + w] = ln_scalars(mean, q * (1.f / 512.f));
    }
    __syncthreads();
    PST(3);
    {
        const float* kq_ = (const float*)smem;
        float* kt = (float*)smem + 8 * 648;
        for (int i = tid; i < 2 * 648; i += 512) {
            const int c = i >= 648, pi = i - 648 * c;
            const float* kp = kq_ + c * (4 * 648) + pi;
            kt[i] = (kp[0] + kp[648]) + (kp[1296] + kp[1944]);
        }
        const int q = l15 >> 2, p = lane & 3;
        {
            const int d0 = 64 * w + 16 * g;
            const lds_char* src = (const lds_char*)smem + P_X + q * 1088 + (d0 + 4 * p) * 2;
            u32x2 r0 = __builtin_bit_cast(u32x2, TRREAD(src)), r1 = __builtin_bit_cast(u32x2, TRREAD(src + 4 * 1088));
            *(u32x4*)(ws + OFF_XT0 + (size_t)(d0 + l15) * (T * 2) + 16 * b) = u32x4{r0[0], r0[1], r1[0], r1[1]};
        }
        {
            const int nt2 = w & 1, k4 = w >> 1;
            const lds_char* src = (const lds_char*)smem + P_W + (32 * k4 + 8 * g + q) * 80 + (16 * nt2 + 4 * p) * 2;
            u32x2 r0 = __builtin_bit_cast(u32x2, TRREAD(src)), r1 = __builtin_bit_cast(u32x2, TRREAD(src + 4 * 80));
            u32x4 ov = {r0[0], r0[1], r1[0], r1[1]};
            const int n16 = 2 * nb + nt2, ks = 4 * kq + k4;
            size_t off;
            if (isq) off = OFF_WF + ((size_t)(((n16 & 31) * 3 + (n16 >> 5)) * 16 + ks) * 64 + lane) * 16;
            else off = OFF_WOF + ((size_t)(n16 * 16 + ks) * 64 + lane) * 16;
            *(u32x4*)(ws + off) = ov;
            float cs = 0.f;
#pragma unroll
            for (int j = 0; j < 4; ++j) cs = dot2(ov[j], 0x3f803f80u, cs);
            cs = xrow16_sum(cs);
            if (g == 0) ((float*)(smem + P_CS))[w * 16 + l15] = cs;
        }
        __syncthreads();
        if (isq && tid < 32) {
            const float* cp = (const float*)(smem + P_CS) + tid;
            ((float*)(ws + OFF_CS))[kq * 1536 + 32 * nb + tid] = (cp[0] + cp[32]) + (cp[64] + cp[96]);
        }
        for (int task = tid; task < 2 * 260; task += 512) {
            const int c = task >= 260, ci = task - 260 * c;
            const int m = 255 - ci;
            int base; unsigned offs;
            if (m < 32) { base = 8 * m; offs = 0x76543210u; }
            else if (m < 64) { base = 256 + 4 * (m - 32); offs = 0x43222100u; }
            else if (m < 128) { base = 384 + 2 * (m - 64); offs = 0x22111000u; }
            else { base = 512 + (m - 128); offs = 0x11100000u | ((unsigned)(m & 1) << 16); }
            if (m < 0) base = 0;
            float v[8];
#pragma unroll
            for (int j = 0; j < 8; ++j) {
                const int i = 7 - j;
                float val = kt[c * 648 + base + ((offs >> (4 * i)) & 15)];
                v[j] = m < 0 ? 0.f : val;
            }
            u32x4 o = {pk(v[0], v[1]), pk(v[2], v[3]), pk(v[4], v[5]), pk(v[6], v[7])};
            *(u32x4*)(ws + OFF_R + (size_t)(2 * b + c) * 4160 + 16 * ci) = o;
        }
    }
    PST(4);
}

constexpr int CV_RC = 4160, CV_RCH = 8 * CV_RC;
constexpr int CV_U0 = 2 * CV_RCH, CV_UPL = 1536, CV_UCH = 4 * CV_UPL;
constexpr int CV_Y0 = CV_U0 + 2 * CV_UCH;
constexpr int CV_YP = 64 * 144;
constexpr int Q_PART = 65536;
constexpr int A_KV = Q_PART + 2048;
constexpr int A_ONES = A_KV + 4 * 20480;
constexpr int MEGA_LDS = CV_Y0 + 8 * CV_YP;
static_assert(MEGA_LDS <= 163840 && A_ONES + 1024 <= MEGA_LDS, "lds");

struct MegaArgs {
    const u16* Dskip;
    u16* out;
    char* ws;
};

constexpr unsigned SPIN_MAX = 2000000u;
DEV u32x4 sanit(u32x4 v) {
#pragma unroll
    for (int j = 0; j < 4; ++j) v[j] = min(v[j], 0xFFFFFFFEu);
    return v;
}
DEV bool has_poison(u32x4 v) { return max(max(v[0], v[1]), max(v[2], v[3])) == POISON; }
#define DMA_VALID(N, SRC, DST, LIVE) { \
    unsigned todo_ = (LIVE); \
    VM0(); \
    for (unsigned spins_ = 0; todo_ != 0u && spins_ < SPIN_MAX; ++spins_) { \
        if (spins_) __builtin_amdgcn_s_sleep(4); \
        _Pragma("unroll") for (int i = 0; i < (N); ++i) \
            if (todo_ & (1u << i)) __builtin_amdgcn_global_load_lds((const unsigned*)(SRC(i)), (lds_u32*)(DST(i)), 16, 0, SC1); \
        VM0(); \
        u32x4 v_[N]; \
        _Pragma("unroll") for (int i = 0; i < (N); ++i) v_[i] = *(const u32x4*)((const char*)(DST(i)) + lane * 16); \
        unsigned bad_ = 0u; \
        _Pragma("unroll") for (int i = 0; i < (N); ++i) bad_ |= __any(has_poison(v_[i])) ? (1u << i) : 0u; \
        todo_ &= bad_; \
    } }
DEV float max3f(float a, float b, float c) { return __builtin_fmaxf(__builtin_fmaxf(a, b), c); }
DEV float max2f(float a, float b) { return __builtin_fmaxf(a, b); }
template <bool EDGE>
DEV u32x4 attn_body(const char* smem, int hw, int kh, int lane, int ab, const bf16x8 Qf, const bf16x8* wo, bf16x8 (&Wo)[16]) {
    const int r32 = lane & 31, hh = lane >> 5;
    const float NEG = -1.0e30f;
    const char* kb = smem + A_KV + hw * 20480 + kh * 2048 + lane * 16;
    const char* vb = (r32 < 16 ? smem + A_KV + hw * 20480 + kh * 2048 + 1024 : smem + A_ONES) + hh * 256 + (r32 & 15) * 16;
    const int vstep = r32 < 16 ? 2048 : 0;
    bf16x8 Kf[9];
#pragma unroll
    for (int j = 0; j < 9; ++j) Kf[j] = *(const bf16x8*)(kb + j * 2048);
    f32x16 zero;
#pragma unroll
    for (int i = 0; i < 16; ++i) zero[i] = 0.f;
    float m0 = NEG, m1 = NEG;
    {
        f32x16 Sn = MFMA32(Kf[0], Qf, zero);
#pragma unroll
        for (int j = 0; j < 9; ++j) {
            f32x16 S = Sn;
            if (j < 8) Sn = MFMA32(Kf[j + 1], Qf, zero);
            SB();
            if (j == 0) {
#pragma unroll
                for (int i = 0; i < 16; ++i) if (!((i & 3) + 8 * (i >> 2) + 4 * hh > r32)) S[i] = NEG;
            }
            if (j == 8) {
#pragma unroll
                for (int i = 0; i < 16; ++i) if ((i & 3) + 8 * (i >> 2) + 4 * hh > r32) S[i] = NEG;
            }
            float a0 = m0, a1 = m1;
#pragma unroll
            for (int i = 0; i < 16; i += 4) { a0 = max3f(a0, S[i], S[i + 1]); a1 = max3f(a1, S[i + 2], S[i + 3]); }
            if (EDGE) { if (ab - 8 + j >= 0) { m0 = a0; m1 = a1; } }
            else { m0 = a0; m1 = a1; }
            SB();
        }
    }
    float m = max2f(m0, m1);
    {
        auto t = __builtin_amdgcn_permlane32_swap(__float_as_uint(m), __float_as_uint(m), false, false);
        m = max2f(__uint_as_float(t[0]), __uint_as_float(t[1]));
    }
    f32x16 nm;
#pragma unroll
    for (int i = 0; i < 16; ++i) nm[i] = -m;
    f32x16 O = zero;
    {
        f32x16 Sn = MFMA32(Kf[0], Qf, nm);
        bf16x8 vn0 = *(const bf16x8*)(vb), vn1 = *(const bf16x8*)(vb + 512);
#pragma unroll
        for (int j = 0; j < 9; ++j) {
            f32x16 S = Sn;
            const bf16x8 v0 = vn0, v1 = vn1;
            if (j < 8) {
                Sn = MFMA32(Kf[j + 1], Qf, nm);
                vn0 = *(const bf16x8*)(vb + (j + 1) * vstep); vn1 = *(const bf16x8*)(vb + (j + 1) * vstep + 512);
                Wo[j] = wo[j * 64];
            }
            SB();
            if (j == 0) {
#pragma unroll
                for (int i = 0; i < 16; ++i) if (!((i & 3) + 8 * (i >> 2) + 4 * hh > r32)) S[i] = NEG;
            }
            if (j == 8) {
#pragma unroll
                for (int i = 0; i < 16; ++i) if ((i & 3) + 8 * (i >> 2) + 4 * hh > r32) S[i] = NEG;
            }
            u32x4 p0, p1;
#pragma unroll
            for (int i = 0; i < 4; ++i) p0[i] = pk(__builtin_amdgcn_exp2f(S[2 * i]), __builtin_amdgcn_exp2f(S[2 * i + 1]));
            if (!EDGE || ab - 8 + j >= 0) O = MFMA32(v0, __builtin_bit_cast(bf16x8, p0), O);
#pragma unroll
            for (int i = 0; i < 4; ++i) p1[i] = pk(__builtin_amdgcn_exp2f(S[8 + 2 * i]), __builtin_amdgcn_exp2f(S[8 + 2 * i + 1]));
            if (!EDGE || ab - 8 + j >= 0) O = MFMA32(v1, __builtin_bit_cast(bf16x8, p1), O);
            SB();
        }
    }
    const float inv = __builtin_amdgcn_rcpf(O[8]);
    const unsigned a0 = pk(O[0] * inv, O[1] * inv), a1 = pk(O[2] * inv, O[3] * inv);
    const unsigned b0 = pk(O[4] * inv, O[5] * inv), b1 = pk(O[6] * inv, O[7] * inv);
    auto sx = __builtin_amdgcn_permlane32_swap(a0, b0, false, false);
    auto sy = __builtin_amdgcn_permlane32_swap(a1, b1, false, false);
    return u32x4{sx[0], sy[0], sx[1], sy[1]};
}

DEV u32x4 attn_fast(const char* smem, int hw, int kh, int lane, const bf16x8 Qf, const bf16x8* wo, bf16x8 (&Wo)[16]) {
    const int r32 = lane & 31, hh = lane >> 5;
    const char* kb = smem + A_KV + hw * 20480 + kh * 2048 + lane * 16;
    const char* vb = (r32 < 16 ? smem + A_KV + hw * 20480 + kh * 2048 + 1024 : smem + A_ONES) + hh * 256 + (r32 & 15) * 16;
    const int vstep = r32 < 16 ? 2048 : 0;
    bf16x8 Kf[9];
#pragma unroll
    for (int j = 0; j < 9; ++j) Kf[j] = *(const bf16x8*)(kb + j * 2048);
    f32x16 zero;
#pragma unroll
    for (int i = 0; i < 16; ++i) zero[i] = 0.f;
    float m0, m1;
    {
        f32x16 Sa = MFMA32(Kf[0], Qf, zero), Sb = MFMA32(Kf[8], Qf, zero);
        f32x16 Sn = MFMA32(Kf[1], Qf, zero);
        SB();
#pragma unroll
        for (int i = 0; i < 16; ++i) Sa[i] = ((i & 3) + 8 * (i >> 2) + 4 * hh > r32) ? Sa[i] : Sb[i];
        m0 = max3f(Sa[0], Sa[1], Sa[2]); m1 = max3f(Sa[3], Sa[4], Sa[5]);
        m0 = max3f(m0, Sa[6], Sa[7]);
#pragma unroll
        for (int i = 8; i < 16; i += 4) { m0 = max3f(m0, Sa[i], Sa[i + 1]); m1 = max3f(m1, Sa[i + 2], Sa[i + 3]); }
        SB();
#pragma unroll
        for (int j = 1; j < 8; ++j) {
            f32x16 S = Sn;
            if (j < 7) Sn = MFMA32(Kf[j + 1], Qf, zero);
            SB();
#pragma unroll
            for (int i = 0; i < 16; i += 4) { m0 = max3f(m0, S[i], S[i + 1]); m1 = max3f(m1, S[i + 2], S[i + 3]); }
            SB();
        }
    }
    float m = max2f(m0, m1);
    {
        auto t = __builtin_amdgcn_permlane32_swap(__float_as_uint(m), __float_as_uint(m), false, false);
        m = max2f(__uint_as_float(t[0]), __uint_as_float(t[1]));
    }
    f32x16 nm;
#pragma unroll
    for (int i = 0; i < 16; ++i) nm[i] = -m;
    f32x16 O = zero;
    {
        f32x16 Sn = MFMA32(Kf[1], Qf, nm);
        bf16x8 vn0 = *(const bf16x8*)(vb + vstep), vn1 = *(const bf16x8*)(vb + vstep + 512);
        f32x16 Sb;
        bf16x8 vb0, vb1;
#pragma unroll
        for (int j = 1; j < 8; ++j) {
            f32x16 S = Sn;
            const bf16x8 v0 = vn0, v1 = vn1;
            const int jn = j < 7 ? j + 1 : 0;
            Sn = MFMA32(Kf[jn], Qf, nm);
            vn0 = *(const bf16x8*)(vb + jn * vstep); vn1 = *(const bf16x8*)(vb + jn * vstep + 512);
            if (j == 7) { Sb = MFMA32(Kf[8], Qf, nm); vb0 = *(const bf16x8*)(vb + 8 * vstep); vb1 = *(const bf16x8*)(vb + 8 * vstep + 512); }
            Wo[j - 1] = wo[(j - 1) * 64];
            if (j == 7) Wo[7] = wo[7 * 64];
            SB();
            u32x4 p0, p1;
#pragma unroll
            for (int i = 0; i < 4; ++i) p0[i] = pk(__builtin_amdgcn_exp2f(S[2 * i]), __builtin_amdgcn_exp2f(S[2 * i + 1]));
            O = MFMA32(v0, __builtin_bit_cast(bf16x8, p0), O);
#pragma unroll
            for (int i = 0; i < 4; ++i) p1[i] = pk(__builtin_amdgcn_exp2f(S[8 + 2 * i]), __builtin_amdgcn_exp2f(S[8 + 2 * i + 1]));
            O = MFMA32(v1, __builtin_bit_cast(bf16x8, p1), O);
            SB();
        }
        u32x4 pa[2], pb[2];
#pragma unroll
        for (int i = 0; i < 8; ++i) {
            const int k0 = ((2 * i) & 3) + 8 * ((2 * i) >> 2) + 4 * hh;
            const bool f0 = k0 > r32, f1 = k0 + 1 > r32;
            const unsigned pp = pk(__builtin_amdgcn_exp2f(f0 ? Sn[2 * i] : Sb[2 * i]), __builtin_amdgcn_exp2f(f1 ? Sn[2 * i + 1] : Sb[2 * i + 1]));
            const unsigned msk = (f0 ? 0xffffu : 0u) | (f1 ? 0xffff0000u : 0u);
            pa[i >> 2][i & 3] = pp & msk;
            pb[i >> 2][i & 3] = pp & ~msk;
        }
        O = MFMA32(vn0, __builtin_bit_cast(bf16x8, pa[0]), O);
        O = MFMA32(vn1, __builtin_bit_cast(bf16x8, pa[1]), O);
        O = MFMA32(vb0, __builtin_bit_cast(bf16x8, pb[0]), O);
        O = MFMA32(vb1, __builtin_bit_cast(bf16x8, pb[1]), O);
    }
    const float inv = __builtin_amdgcn_rcpf(O[8]);
    const unsigned a0 = pk(O[0] * inv, O[1] * inv), a1 = pk(O[2] * inv, O[3] * inv);
    const unsigned b0 = pk(O[4] * inv, O[5] * inv), b1 = pk(O[6] * inv, O[7] * inv);
    auto sx = __builtin_amdgcn_permlane32_swap(a0, b0, false, false);
    auto sy = __builtin_amdgcn_permlane32_swap(a1, b1, false, false);
    return u32x4{sx[0], sy[0], sx[1], sy[1]};
}

template <int layer>
DEV void layer_body(const MegaArgs& a, char* smem) {
    char* ws = a.ws;
    const int tid0 = threadIdx.x;
    const int w = __builtin_amdgcn_readfirstlane(tid0 >> 6);
    const int b = blockIdx.x;
    const int tt = 4 * (b & 7) + ((b >> 3) & 3), hg = b >> 5;
    const int hw = w & 3, kh = w >> 2;
    const int h = 4 * hg + hw;
    const __amdgpu_buffer_rsrc_t rs = __builtin_amdgcn_make_buffer_rsrc((void*)ws, 0, WS_USED, 0x00020000);
    {
        const unsigned xin = layer == 0 ? OFF_XT0 : OFF_XT2;
        const unsigned xmid = layer == 0 ? OFF_XT1 : OFF_XT3;
        const unsigned kvb = OFF_KV + layer * 4 * MB;
        const unsigned ofb = OFF_OF + layer * 2 * MB;
        bf16x8 Wk[16], Wv[16];
        f32x4 csq = {0.f, 0.f, 0.f, 0.f}, csk = {0.f, 0.f, 0.f, 0.f};
        float csv = 0.f;
        {
            PHASE_IDS();
            const int c = tid >> 8, ti = tid & 255;
            u32x4 xv, sv[4], s0v[2], pf[8];
            if (layer == 0) {
                xv = *(const u32x4*)(ws + xin + (size_t)(2 * b + c) * (T * 2) + 16 * ti);
                s0v[0] = *(const u32x4*)(ws + OFF_ST0 + 32 * tid);
                s0v[1] = *(const u32x4*)(ws + OFF_ST0 + 32 * tid + 16);
            }
            const float dk = bf2f(a.Dskip[2 * b + c]);
            {
                const int ci2 = 256 + (tid & 3), c2 = (tid >> 2) & 1;
                const u32x4* src = (const u32x4*)(ws + OFF_R + (size_t)(2 * b + c) * 4160 + 16 * ti);
                const u32x4* src2 = (const u32x4*)(ws + OFF_R + (size_t)(2 * b + c2) * 4160 + 16 * (ci2 > 258 ? 258 : ci2));
                u32x4 v0 = src[0], v1 = src[1], z0 = src2[0], z1 = src2[1];
#pragma unroll
                for (int rep = 0; rep < 2; ++rep) {
                    if (rep == 1 && tid >= 8) break;
                    unsigned dw[8];
#pragma unroll
                    for (int j = 0; j < 4; ++j) { dw[j] = rep ? z0[j] : v0[j]; dw[4 + j] = rep ? z1[j] : v1[j]; }
                    char* dst = smem + (rep ? c2 * CV_RCH + 16 * (ci2 > 258 ? 258 : ci2) : c * CV_RCH + 16 * ti);
#pragma unroll
                    for (int s = 0; s < 8; ++s) {
                        u32x4 o;
#pragma unroll
                        for (int d = 0; d < 4; ++d) {
                            const int k = (s >> 1) + d;
                            o[d] = (s & 1) ? __builtin_amdgcn_alignbyte(dw[k + 1], dw[k], 2) : dw[k];
                        }
                        *(u32x4*)(dst + s * CV_RC) = o;
                    }
                }
                if (tid < 256) *(u32x4*)(smem + CV_U0 + (tid >> 5) * CV_UPL + 16 * (tid & 31)) = u32x4{0u, 0u, 0u, 0u};
            }
            STAMP(16 * layer + 1);
            f32x4* stl = (f32x4*)(smem + CV_Y0);
            if (layer != 0) {
                u32x4 av[4];
                VM0();
                bool bad = true;
                for (unsigned spins = 0; spins < SPIN_MAX; ++spins) {
                    asm volatile("" ::: "memory");
                    if (bad) {
                        xv = LD128(rs, xin + (unsigned)(2 * b + c) * (T * 2) + 16 * ti);
                        bad = has_poison(xv);
                    }
                    if (!__any(bad)) break;
                    __builtin_amdgcn_s_sleep(4);
                }
                BAR();
                bad = true;
                for (unsigned spins = 0; spins < SPIN_MAX; ++spins) {
                    asm volatile("" ::: "memory");
                    if (bad) {
#pragma unroll
                        for (int j = 0; j < 4; ++j) av[j] = LD128(rs, OFF_ACC + 8192 * j + 16 * tid);
                        bad = false;
#pragma unroll
                        for (int j = 0; j < 4; ++j) bad |= (av[j][1] >> 26) != 32u || (av[j][3] >> 26) != 32u;
                    }
                    if (!__any(bad)) break;
                    __builtin_amdgcn_s_sleep(8);
                }
                STAMP(16 + 2);
#pragma unroll
                for (int j = 0; j < 4; ++j) {
                    double s1 = (double)(int)((av[j][1] & 0x03FFFFFFu) - 0x01000000u) * 4294967296.0 + (double)av[j][0];
                    double s2 = (double)(int)((av[j][3] & 0x03FFFFFFu) - 0x01000000u) * 4294967296.0 + (double)av[j][2];
                    double mean = s1 * (1.0 / (1073741824.0 * 512.0));
                    double var = s2 * (1.0 / (16777216.0 * 512.0)) - mean * mean;
                    ((f2_t*)stl)[512 * j + tid] = ln_scalars((float)mean, fmaxf((float)var, 0.f));
                }
            } else {
                stl[2 * tid] = __builtin_bit_cast(f32x4, s0v[0]);
                stl[2 * tid + 1] = __builtin_bit_cast(f32x4, s0v[1]);
            }
            BAR();
#pragma unroll
            for (int j = 0; j < 4; ++j) sv[j] = __builtin_bit_cast(u32x4, stl[4 * ti + j]);
            u32x4 uv;
#pragma unroll
            for (int j = 0; j < 4; ++j) {
                float u0 = rbf(bflo(xv[j]) - __uint_as_float(sv[j][0])) * __uint_as_float(sv[j][1]);
                float u1 = rbf(bfhi(xv[j]) - __uint_as_float(sv[j][2])) * __uint_as_float(sv[j][3]);
                uv[j] = pk(u0, u1);
            }
            *(u32x4*)(smem + CV_U0 + c * CV_UCH + (ti & 3) * CV_UPL + 16 * (32 + (ti >> 2))) = uv;
            BAR();
            STAMP(16 * layer + 3);
            {
                const int cw = w >> 2, kk = w & 1, par = (w >> 1) & 1;
                const int r = lane & 31, hh = lane >> 5;
                const int rho = r >> 3, cc = r & 7;
                const char* pa = smem + cw * CV_RCH + (7 - cc) * CV_RC + 16 * (255 + 2 * kk - rho + hh) - 64 * par;
                const char* pb = smem + CV_U0 + cw * CV_UCH + (2 * kk + hh) * CV_UPL + 16 * (32 + r) - 16 * par;
                const bf16x8* wf = (const bf16x8*)(ws + OFF_WF) + (size_t)(h * 3) * 16 * 64 + lane;
                const float* cs = (const float*)(ws + OFF_CS);
                f32x4 cq[4], ck[4];
                float cv[4];
                f32x16 acc0, acc1;
#pragma unroll
                for (int i = 0; i < 16; ++i) { acc0[i] = 0.f; acc1[i] = 0.f; }
                bf16x8 a0 = *(const bf16x8*)(pa), a1 = *(const bf16x8*)(pa - 2048);
                bf16x8 b0 = *(const bf16x8*)(pb), b1 = *(const bf16x8*)(pb + 512);
#pragma unroll
                for (int it = 0; it < 16; ++it) {
                    bf16x8 na0, na1, nb0, nb1;
                    if (it < 15) {
                        na0 = *(const bf16x8*)(pa - 128 * (it + 1));
                        na1 = *(const bf16x8*)(pa - 128 * (it + 1) - 2048);
                        nb0 = *(const bf16x8*)(pb - 32 * (it + 1));
                        nb1 = *(const bf16x8*)(pb - 32 * (it + 1) + 512);
                    }
                    {
                        const int id = it, i = id >> 1;
                        if (id & 1) Wv[i] = wf[(32 + ((i + 4 * hw) & 15)) * 64];
                        else Wk[i] = wf[(16 + ((i + 4 * hw) & 15)) * 64];
                    }
                    if (it >= 12) {
                        const int kq = it - 12;
                        cq[kq] = *(const f32x4*)(cs + kq * 1536 + h * 16 + 4 * g);
                        ck[kq] = *(const f32x4*)(cs + kq * 1536 + 512 + h * 16 + 4 * g);
                        cv[kq] = cs[kq * 1536 + 1024 + h * 16 + l15];
                    }
                    SB();
                    acc0 = MFMA32(a0, b0, acc0);
                    acc1 = MFMA32(a0, b1, acc1);
                    acc1 = MFMA32(a1, b0, acc1);
                    SB();
                    if (it < 15) { a0 = na0; a1 = na1; b0 = nb0; b1 = nb1; }
                }
                char* yp = smem + CV_Y0 + w * CV_YP + r * 144 + 16 * hh;
#pragma unroll
                for (int q = 0; q < 4; ++q) {
                    f32x4 v0 = {acc0[4 * q], acc0[4 * q + 1], acc0[4 * q + 2], acc0[4 * q + 3]};
                    f32x4 v1 = {acc1[4 * q], acc1[4 * q + 1], acc1[4 * q + 2], acc1[4 * q + 3]};
                    *(f32x4*)(yp + 32 * q) = v0;
                    *(f32x4*)(yp + 32 * 144 + 32 * q) = v1;
                }
                SB();
                csq = (cq[0] + cq[1]) + (cq[2] + cq[3]);
                csk = (ck[0] + ck[1]) + (ck[2] + ck[3]);
                csv = (cv[0] + cv[1]) + (cv[2] + cv[3]);
            }
            STAMP(16 * layer + 4);
            BAR();
            {
                const char* y0 = smem + CV_Y0 + (4 * c) * CV_YP + (ti >> 2) * 144 + (ti & 3) * 32;
                f32x4 pp[8];
#pragma unroll
                for (int p = 0; p < 4; ++p) { pp[2 * p] = *(const f32x4*)(y0 + p * CV_YP); pp[2 * p + 1] = *(const f32x4*)(y0 + p * CV_YP + 16); }
                f32x4 ya = (pp[0] + pp[2]) + (pp[4] + pp[6]), yb = (pp[1] + pp[3]) + (pp[5] + pp[7]);
                float y[8] = {ya[0], ya[1], ya[2], ya[3], yb[0], yb[1], yb[2], yb[3]};
                u32x4 o;
#pragma unroll
                for (int j = 0; j < 4; ++j) {
                    float s0 = rbf(rbf(y[2 * j]) + rbf(bflo(uv[j]) * dk));
                    float s1 = rbf(rbf(y[2 * j + 1]) + rbf(bfhi(uv[j]) * dk));
                    o[j] = pk(bflo(xv[j]) + s0, bfhi(xv[j]) + s1);
                }
                ST128(sanit(o), rs, xmid + (unsigned)(2 * b + c) * (T * 2) + 16 * ti);
                SB();
                const bf16x8* wf = (const bf16x8*)(ws + OFF_WF) + (size_t)(h * 3) * 16 * 64 + lane;
#pragma unroll
                for (int id = 16; id < 32; ++id) {
                    const int i = id >> 1;
                    if (id & 1) Wv[i] = wf[(32 + ((i + 4 * hw) & 15)) * 64];
                    else Wk[i] = wf[(16 + ((i + 4 * hw) & 15)) * 64];
                }
            }
            BAR();
            STAMP(16 * layer + 5);
        }
        u32x2 xres[2];
        bf16x8 Qf;
        {
            PHASE_IDS();
            {
                const int rr = lane >> 3, chp = lane & 7;
                const int f = ((rr >> 1) & 1) | ((w & 1) << 1);
                const char* src0 = ws + xmid + (size_t)(8 * w + rr) * (T * 2) + 128 * tt + 16 * (chp ^ (2 * f));
#define XSRC(i) (src0 + (size_t)(i) * (64 * T * 2))
#define XDST(i) (smem + 1024 * (w + 8 * (i)))
                DMA_VALID(8, XSRC, XDST, 0xFFu);
            }
            BAR();
            STAMP(16 * layer + 7);
            if (layer == 0) {
                const u32x4 pz = {POISON, POISON, POISON, POISON};
                ST128(pz, rs, OFF_XT3 + (unsigned)(b * 512 + tid) * 16);
                ST128(pz, rs, OFF_KV + 4 * MB + (unsigned)(b * 1024 + tid) * 16);
                ST128(pz, rs, OFF_KV + 4 * MB + (unsigned)(b * 1024 + 512 + tid) * 16);
                ST128(pz, rs, OFF_OF + 2 * MB + (unsigned)(b * 512 + tid) * 16);
            }
            {
                const int d = 64 * hg + 16 * hw + l15;
                const int f = ((d >> 1) & 1) | (((d >> 3) & 1) << 1);
#pragma unroll
                for (int i = 0; i < 2; ++i)
                    xres[i] = *(const u32x2*)(smem + d * 128 + 16 * ((4 * kh + 2 * i + (g >> 1)) ^ (2 * f)) + 8 * (g & 1));
            }
            const int q = l15 >> 2, p = lane & 3;
            const int f = ((q >> 1) & 1) | ((g & 1) << 1);
            const lds_char* lbase = (const lds_char*)smem + (8 * g + q) * 128 + 8 * (p & 1);
            const bf16x8* wq = (const bf16x8*)(ws + OFF_WF) + (size_t)(h * 3) * 16 * 64 + lane;
            bf16x8 Wq[16];
#define TRLD(dst, i_) { const lds_char* lb_ = lbase + (((i_) + 4 * hw) & 15) * 4096; _Pragma("unroll") for (int ti = 0; ti < 2; ++ti) { \
                const int ch = (2 * (2 * kh + ti) + (p >> 1)) ^ (2 * f); \
                v4s x0 = TRREAD(lb_ + 16 * ch); \
                v4s x1 = TRREAD(lb_ + 512 + 16 * ch); \
                dst[ti] = bf16x8{x0[0], x0[1], x0[2], x0[3], x1[0], x1[1], x1[2], x1[3]}; } }
            f32x4 ak[2], av[2];
            f32x4 d1[2] = {f32x4{0.f, 0.f, 0.f, 0.f}, f32x4{0.f, 0.f, 0.f, 0.f}}, d2[2] = {f32x4{0.f, 0.f, 0.f, 0.f}, f32x4{0.f, 0.f, 0.f, 0.f}};
            const bf16x8 ones = {0x3f80, 0x3f80, 0x3f80, 0x3f80, 0x3f80, 0x3f80, 0x3f80, 0x3f80};
#pragma unroll
            for (int i = 0; i < 2; ++i) { ak[i] = f32x4{0.f, 0.f, 0.f, 0.f}; av[i] = f32x4{0.f, 0.f, 0.f, 0.f}; }
            f2_t* part = (f2_t*)(smem + Q_PART);
            bf16x8 xr[3][2];
            TRLD(xr[0], 0); TRLD(xr[1], 1); TRLD(xr[2], 2);
#pragma unroll
            for (int i = 0; i < 16; ++i) {
                Wq[i] = wq[((i + 4 * hw) & 15) * 64];
                SB();
#pragma unroll
                for (int ti = 0; ti < 2; ++ti) {
                    ak[ti] = MFMA16(Wk[i], xr[i % 3][ti], ak[ti]);
                    av[ti] = MFMA16(xr[i % 3][ti], Wv[i], av[ti]);
                    if (i < 4) {
                        d1[ti] = MFMA16(ones, xr[i % 3][ti], d1[ti]);
                        d2[ti] = MFMA16(xr[i % 3][ti], xr[i % 3][ti], d2[ti]);
                    }
                    if (i == 6 + ti) {
                        const int rr = l15 & 3;
                        const float a2 = rr == 0 ? d2[ti][0] : rr == 1 ? d2[ti][1] : rr == 2 ? d2[ti][2] : d2[ti][3];
                        if (g == (l15 >> 2)) part[w * 32 + 16 * ti + l15] = f2_t{d1[ti][0], a2};
                    }
                }
                SB();
                if (i + 3 < 16) { TRLD(xr[i % 3], i + 3); }
            }
            BAR();
            STAMP(16 * layer + 8);
            f2_t st;
            {
                float x1 = 0.f, x2 = 0.f;
#pragma unroll
                for (int i = 0; i < 4; ++i) { f2_t v = part[(4 * kh + i) * 32 + (lane & 31)]; x1 += v[0]; x2 += v[1]; }
                float mean = x1 * (1.f / 512.f);
                float var = x2 * (1.f / 512.f) - mean * mean;
                st = ln_scalars(mean, fmaxf(var, 0.f));
            }
            float m0[2], r0[2];
            {
                u32x4 ko, vo;
#pragma unroll
                for (int i = 0; i < 2; ++i) {
                    m0[i] = __shfl(st[0], 16 * i + l15); r0[i] = __shfl(st[1], 16 * i + l15);
                    float kv[4], vv[4];
#pragma unroll
                    for (int r = 0; r < 4; ++r) {
                        kv[r] = (ak[i][r] - m0[i] * csk[r]) * r0[i];
                        const float m1 = __shfl(st[0], 16 * i + 4 * g + r), r1 = __shfl(st[1], 16 * i + 4 * g + r);
                        vv[r] = (av[i][r] - m1 * csv) * r1;
                    }
                    ko[2 * i] = pk(kv[0], kv[1]); ko[2 * i + 1] = pk(kv[2], kv[3]);
                    vo[2 * i] = pk(vv[0], vv[1]); vo[2 * i + 1] = pk(vv[2], vv[3]);
                }
                auto kx = __builtin_amdgcn_permlane16_swap(ko[0], ko[2], false, false);
                auto ky = __builtin_amdgcn_permlane16_swap(ko[1], ko[3], false, false);
                ko = sanit(u32x4{kx[0], ky[0], kx[1], ky[1]});
                auto vx = __builtin_amdgcn_permlane32_swap(vo[0], vo[2], false, false);
                auto vy = __builtin_amdgcn_permlane32_swap(vo[1], vo[3], false, false);
                vo = sanit(u32x4{vx[0], vy[0], vx[1], vy[1]});
                const unsigned go = kvb + (unsigned)(h * 64 + 2 * tt + kh) * 2048 + lane * 16;
                ST128(ko, rs, go);
                ST128(vo, rs, go + 1024);
                char* lo = smem + A_KV + hw * 20480 + (8 + kh) * 2048 + lane * 16;
                *(u32x4*)(lo) = ko;
                *(u32x4*)(lo + 1024) = vo;
                if (w == 0) *(u32x4*)(smem + A_ONES + lane * 16) = u32x4{0x3f803f80u, 0x3f803f80u, 0x3f803f80u, 0x3f803f80u};
            }
            STAMP(16 * layer + 6);
            f32x4 aq[2][2];
#pragma unroll
            for (int i = 0; i < 2; ++i) { aq[i][0] = f32x4{0.f, 0.f, 0.f, 0.f}; aq[i][1] = f32x4{0.f, 0.f, 0.f, 0.f}; }
            bf16x8 xq[6][2];
#pragma unroll
            for (int i = 0; i < 6; ++i) { TRLD(xq[i], i); }
#pragma unroll
            for (int i = 0; i < 16; ++i) {
                SB();
#pragma unroll
                for (int ti = 0; ti < 2; ++ti) aq[ti][i & 1] = MFMA16(Wq[i], xq[i % 6][ti], aq[ti][i & 1]);
                SB();
                if (i + 6 < 16) { TRLD(xq[i % 6], i + 6); }
            }
            {
                const float qs = 0.25f * 1.4426950408889634f;
                u32x4 qo;
#pragma unroll
                for (int i = 0; i < 2; ++i) {
                    f32x4 v = aq[i][0] + aq[i][1];
                    float qv[4];
#pragma unroll
                    for (int r = 0; r < 4; ++r) qv[r] = rbf((v[r] - m0[i] * csq[r]) * r0[i]) * qs;
                    qo[2 * i] = pk(qv[0], qv[1]); qo[2 * i + 1] = pk(qv[2], qv[3]);
                }
                auto qx = __builtin_amdgcn_permlane16_swap(qo[0], qo[2], false, false);
                auto qy = __builtin_amdgcn_permlane16_swap(qo[1], qo[3], false, false);
                Qf = __builtin_bit_cast(bf16x8, (u32x4{qx[0], qy[0], qx[1], qy[1]}));
            }
            STAMP(16 * layer + 10);
        }
        bf16x8 Wo[16];
        {
            PHASE_IDS();
            {
                const int P0 = 2 * tt - 8 + 4 * kh;
                const char* src0 = ws + kvb + (size_t)(h * 64 + P0) * 2048 + lane * 16;
                char* dst0 = smem + A_KV + hw * 20480 + 4 * kh * 2048;
                const unsigned live = P0 >= 0 ? 0xFFu : (P0 <= -4 ? 0u : (0xFFu << (2 * (-P0))) & 0xFFu);
#define KSRC(i) (src0 + (i) * 1024)
#define KDST(i) (dst0 + (i) * 1024)
                DMA_VALID(8, KSRC, KDST, live);
            }
            BAR();
            STAMP(16 * layer + 12);
            const bf16x8* wo = (const bf16x8*)(ws + OFF_WOF) + (size_t)((4 * hg + hw) * 16) * 64 + lane;
            const int ab = 2 * tt + kh;
            u32x4 o = ab < 8 ? attn_body<true>(smem, hw, kh, lane, ab, Qf, wo, Wo) : attn_fast(smem, hw, kh, lane, Qf, wo, Wo);
            {
                const int tile = 2 * ab + (g & 1);
                const int gp = 2 * (h & 1) + (g >> 1);
                ST128(sanit(o), rs, ofb + (unsigned)((tile * 16 + (h >> 1)) * 64 + l15 + 16 * gp) * 16);
                SB();
#pragma unroll
                for (int ks = 8; ks < 16; ++ks) Wo[ks] = wo[ks * 64];
            }
            STAMP(16 * layer + 9);
            BAR();
            STAMP(16 * layer + 13);
        }
        {
            PHASE_IDS();
            STAMP(16 * layer + 14);
            {
                const char* src0 = ws + ofb + (size_t)(4 * tt * 16 + w) * 1024 + lane * 16;
#define OSRC(i) (src0 + (i) * 8192)
                DMA_VALID(8, OSRC, XDST, 0xFFu);
            }
            BAR();
            STAMP(16 * layer + 11);
            f32x4 acc[2][2];
#pragma unroll
            for (int i = 0; i < 2; ++i) { acc[i][0] = f32x4{0.f, 0.f, 0.f, 0.f}; acc[i][1] = f32x4{0.f, 0.f, 0.f, 0.f}; }
            {
                const char* op = smem + (2 * kh) * 16384 + lane * 16;
                bf16x8 of[2][8], og[2][8];
#pragma unroll
                for (int ks = 0; ks < 8; ++ks)
#pragma unroll
                    for (int i = 0; i < 2; ++i) of[i][ks] = *(const bf16x8*)(op + (i * 16 + ks) * 1024);
                SB();
#pragma unroll
                for (int ks = 0; ks < 8; ++ks) {
#pragma unroll
                    for (int i = 0; i < 2; ++i) og[i][ks] = *(const bf16x8*)(op + (i * 16 + 8 + ks) * 1024);
#pragma unroll
                    for (int i = 0; i < 2; ++i) acc[i][ks & 1] = MFMA16(of[i][ks], Wo[ks], acc[i][ks & 1]);
                    SB();
                }
#pragma unroll
                for (int ks = 0; ks < 8; ++ks)
#pragma unroll
                    for (int i = 0; i < 2; ++i) acc[i][ks & 1] = MFMA16(og[i][ks], Wo[8 + ks], acc[i][ks & 1]);
            }
            u32x2 o[2];
#pragma unroll
            for (int i = 0; i < 2; ++i) {
                f32x4 v = acc[i][0] + acc[i][1];
                o[i][0] = pk(bflo(xres[i][0]) + rbf(v[0]), bfhi(xres[i][0]) + rbf(v[1]));
                o[i][1] = pk(bflo(xres[i][1]) + rbf(v[2]), bfhi(xres[i][1]) + rbf(v[3]));
            }
            if (layer == 0) {
                float sel = 0.f;
#pragma unroll
                for (int i = 0; i < 2; ++i)
#pragma unroll
                    for (int r = 0; r < 4; ++r) {
                        const float x = (r & 1) ? bfhi(o[i][r >> 1]) : bflo(o[i][r >> 1]);
                        const float a1 = row16_sum(x), a2 = row16_sum(x * x);
                        if (l15 == 2 * (4 * i + r)) sel = a1;
                        if (l15 == 2 * (4 * i + r) + 1) sel = a2;
                    }
                {
                    const int e = l15 >> 1;
                    const int t = 64 * tt + 32 * kh + 16 * (e >> 2) + 4 * g + (e & 3);
                    const float sc = (l15 & 1) ? 16777216.0f : 1073741824.0f;
                    long long iv = (long long)__builtin_rintf(fminf(fmaxf(sel * sc, -1.0e15f), 1.0e15f)) + (1ll << 51) + (1ll << 58);
                    atomicAdd((unsigned long long*)(ws + OFF_ACC) + (size_t)t * 2 + (l15 & 1), (unsigned long long)iv);
                }
                SB();
                {
                    auto sx = __builtin_amdgcn_permlane16_swap(o[0][0], o[1][0], false, false);
                    auto sy = __builtin_amdgcn_permlane16_swap(o[0][1], o[1][1], false, false);
                    u32x4 ov = {sx[0], sy[0], sx[1], sy[1]};
                    ST128(sanit(ov), rs, OFF_XT2 + (unsigned)(64 * hg + 16 * hw + l15) * (T * 2) + 128 * tt + 64 * kh + 32 * (g & 1) + 16 * (g >> 1));
                }
                BAR();
            } else {
                BAR();
                u16* img = (u16*)smem;
#pragma unroll
                for (int i = 0; i < 2; ++i)
#pragma unroll
                    for (int r = 0; r < 4; ++r)
                        img[(32 * kh + 16 * i + 4 * g + r) * 72 + 16 * hw + l15] = (u16)((r & 1) ? (o[i][r >> 1] >> 16) : (o[i][r >> 1] & 0xffff));
                BAR();
                const int t = tid >> 3, nc = tid & 7;
                u32x4 r = *(const u32x4*)(img + t * 72 + 8 * nc);
                *(u32x4*)(a.out + (size_t)(64 * tt + t) * D + 64 * hg + 8 * nc) = r;
            }
            STAMP(16 * layer + 15);
        }
    }
}

__global__ __launch_bounds__(512, 2) void k_mega(MegaArgs a) {
    extern __shared__ __attribute__((aligned(16))) char smem[];
    char* ws = a.ws;
    STAMP(0);
    layer_body<0>(a, smem);
    layer_body<1>(a, smem);
}

extern "C" void kernel_launch(void* const* d_in, const int* in_sizes, int n_in, void* d_out, int out_size, void* d_ws,
                              size_t ws_size, hipStream_t stream) {
    char* ws = (char*)d_ws;
    PrepArgs pa;
    pa.x = (const u16*)d_in[0];
    pa.wqkv = (const u16*)d_in[10];
    pa.wo = (const u16*)d_in[11];
    pa.Are = (const u16*)d_in[1];
    pa.Aim = (const u16*)d_in[2];
    pa.Bre = (const u16*)d_in[3];
    pa.Bim = (const u16*)d_in[4];
    pa.Cre = (const u16*)d_in[5];
    pa.Cim = (const u16*)d_in[6];
    pa.logdt = (const u16*)d_in[7];
    pa.ws = ws;
    MegaArgs ma;
    ma.Dskip = (const u16*)d_in[8];
    ma.out = (u16*)d_out;
    ma.ws = ws;
    (void)hipFuncSetAttribute((const void*)k_mega, hipFuncAttributeMaxDynamicSharedMemorySize, MEGA_LDS);
    k_prep<<<256, 512, 0, stream>>>(pa);
    k_mega<<<256, 512, MEGA_LDS, stream>>>(ma);
}
```

```cpp
#include <hip/hip_runtime.h>
#include <hip/hip_bf16.h>
#include <stdint.h>

typedef short bf16x8 __attribute__((ext_vector_type(8)));
typedef short bf16x4 __attribute__((ext_vector_type(4)));
typedef float f32x4 __attribute__((ext_vector_type(4)));
typedef float f32x16 __attribute__((ext_vector_type(16)));
typedef unsigned u32x4 __attribute__((ext_vector_type(4)));
typedef unsigned u32x2 __attribute__((ext_vector_type(2)));
typedef __bf16 bf2_t __attribute__((ext_vector_type(2)));
typedef float f2_t __attribute__((ext_vector_type(2)));
typedef unsigned short u16;
typedef __attribute__((address_space(3))) char lds_char;
typedef __attribute__((address_space(3))) unsigned lds_u32;

#define DEV __device__ __forceinline__
#define STAMP(i) do {} while (0)

#define PST(i) do {} while (0)
constexpr int T = 2048, D = 512, NM = 128;

constexpr unsigned MB = 1024 * 1024;
constexpr unsigned OFF_XT0 = 0, OFF_XT1 = 2 * MB, OFF_XT2 = 4 * MB, OFF_XT3 = 6 * MB;
constexpr unsigned OFF_WF = 8 * MB;
constexpr unsigned OFF_WOF = 9 * MB + 512 * 1024;
constexpr unsigned OFF_CS = 10 * MB;
constexpr unsigned OFF_ST0 = 10 * MB + 64 * 1024;
constexpr unsigned OFF_ACC = 10 * MB + 128 * 1024;
constexpr unsigned OFF_FLAGS = 10 * MB + 192 * 1024;
constexpr unsigned OFF_KV = 12 * MB;
constexpr unsigned OFF_OF = 20 * MB;
constexpr unsigned OFF_R = 24 * MB;
constexpr unsigned WS_USED = 28 * MB;

DEV float bf2f(unsigned v) { return __uint_as_float(v << 16); }
DEV float bflo(unsigned v) { return __uint_as_float(v << 16); }
DEV float bfhi(unsigned v) { return __uint_as_float(v & 0xffff0000u); }
DEV unsigned pk(float lo, float hi) {
    f2_t v = {lo, hi};
    return __builtin_bit_cast(unsigned, __builtin_convertvector(v, bf2_t));
}
DEV float rbf(float x) { return __uint_as_float(pk(x, 0.f) << 16); }
DEV float dot2(unsigned a, unsigned b, float acc) {
    return __builtin_amdgcn_fdot2_f32_bf16(__builtin_bit_cast(bf2_t, a), __builtin_bit_cast(bf2_t, b), acc, false);
}
DEV float dpp_ror8(float x) { return __builtin_bit_cast(float, __builtin_amdgcn_mov_dpp(__builtin_bit_cast(int, x), 0x128, 0xf, 0xf, true)); }
DEV float xrow16_max(float x) {
    auto s = __builtin_amdgcn_permlane16_swap(__float_as_uint(x), __float_as_uint(x), false, false);
    x = fmaxf(__uint_as_float(s[0]), __uint_as_float(s[1]));
    auto t = __builtin_amdgcn_permlane32_swap(__float_as_uint(x), __float_as_uint(x), false, false);
    return fmaxf(__uint_as_float(t[0]), __uint_as_float(t[1]));
}
DEV float xrow16_sum(float x) {
    auto s = __builtin_amdgcn_permlane16_swap(__float_as_uint(x), __float_as_uint(x), false, false);
    x = __uint_as_float(s[0]) + __uint_as_float(s[1]);
    auto t = __builtin_amdgcn_permlane32_swap(__float_as_uint(x), __float_as_uint(x), false, false);
    return __uint_as_float(t[0]) + __uint_as_float(t[1]);
}

#define MFMA16(a, b, c) __builtin_amdgcn_mfma_f32_16x16x32_bf16(a, b, c, 0, 0, 0)
#define MFMA32(a, b, c) __builtin_amdgcn_mfma_f32_32x32x16_bf16(a, b, c, 0, 0, 0)
#define MFMA16K16(a, b, c) __builtin_amdgcn_mfma_f32_16x16x16bf16_1k(a, b, c, 0, 0, 0)
#define SC1 16
#define LD128(rs, off) __builtin_amdgcn_raw_buffer_load_b128(rs, off, 0, SC1)
#define ST128(v, rs, off) __builtin_amdgcn_raw_buffer_store_b128(v, rs, off, 0, SC1)
#define VM0() asm volatile("s_waitcnt vmcnt(0)" ::: "memory")
typedef short v4s __attribute__((ext_vector_type(4)));
#define TRREAD(p) __builtin_amdgcn_ds_read_tr16_b64_v4i16((__attribute__((address_space(3))) v4s*)(p))
#define SB() __builtin_amdgcn_sched_barrier(0)
#define BAR() asm volatile("s_waitcnt lgkmcnt(0)\n\ts_barrier" ::: "memory")
#define PHASE_IDS() int tid = tid0; asm volatile("" : "+v"(tid)); const int lane = tid & 63, g = lane >> 4, l15 = lane & 15; (void)g; (void)l15;

DEV f2_t cexp_l(f2_t ef, float l) {
    f2_t t = ef * l;
    float e = __builtin_amdgcn_exp2f(t[0]);
    float fr = __builtin_amdgcn_fractf(t[1]);
    f2_t cs = {__builtin_amdgcn_cosf(fr), __builtin_amdgcn_sinf(fr)};
    return cs * e;
}
DEV void split2(f2_t v, unsigned& hi, unsigned& lo) {
    hi = pk(v[0], v[1]);
    f2_t hf = {bflo(hi), bfhi(hi)};
    f2_t r = v - hf;
    lo = pk(r[0], r[1]);
}
DEV float hival(int row) {
    int v;
    if (row < 16) v = (row & 7) + 128 * (row >> 3);
    else if (row < 24) { int i = row - 16; v = 256 + 2 * (i & 3) + 128 * (i >> 2); }
    else if (row < 32) { int i = row - 24; v = 512 + 4 * (i & 1) + 128 * (i >> 1); }
    else if (row < 40) v = 1024 + 128 * (row - 32);
    else if (row == 40) v = 2048;
    else v = 0;
    return (float)v;
}
DEV f2_t ln_scalars(float mean, float var) {
    float mb = rbf(mean);
    float vb = rbf(var);
    float sb = rbf(__builtin_amdgcn_sqrtf(rbf(vb + 1e-5f)));
    f2_t r = {mb, __builtin_amdgcn_rcpf(sb)};
    return r;
}

template <int CTRL> DEV float dppf(float x) { return __builtin_bit_cast(float, __builtin_amdgcn_mov_dpp(__builtin_bit_cast(int, x), CTRL, 0xf, 0xf, true)); }
DEV float row16_sum(float x) {
    x += dppf<0xB1>(x);
    x += dppf<0x4E>(x);
    x += dppf<0x124>(x);
    x += dppf<0x128>(x);
    return x;
}

constexpr unsigned POISON = 0xFFFFFFFFu;
struct PrepArgs {
    const u16 *x, *wqkv, *wo, *Are, *Aim, *Bre, *Bim, *Cre, *Cim, *logdt;
    char* ws;
};
__global__ __launch_bounds__(512) void k_prep(PrepArgs a) {
    char* __restrict__ ws = a.ws;
    __shared__ __attribute__((aligned(16))) char smem[8 * 648 * 4 + 2 * 648 * 4 + 64 * 144 + 128 * 80 + 1024];
    constexpr int P_X = 10 * 648 * 4, P_W = P_X + 64 * 144, P_CS = P_W + 128 * 80;
    const int tid = threadIdx.x, lane = tid & 63, w = tid >> 6;
    const int b = blockIdx.x;
    const int g = lane >> 4, l15 = lane & 15;
    PST(0);
    const int chl = w >> 2, kq4 = w & 3, ch = 2 * b + chl;
    u32x2 vAr[2], vAi[2], vBr[2], vBi[2], vCr[2], vCi[2];
#pragma unroll
    for (int ks = 0; ks < 2; ++ks) {
        const int n0 = ch * NM + 32 * kq4 + 16 * ks + 4 * g;
        vAr[ks] = *(const u32x2*)(a.Are + n0); vAi[ks] = *(const u32x2*)(a.Aim + n0);
        vBr[ks] = *(const u32x2*)(a.Bre + n0); vBi[ks] = *(const u32x2*)(a.Bim + n0);
        vCr[ks] = *(const u32x2*)(a.Cre + n0); vCi[ks] = *(const u32x2*)(a.Cim + n0);
    }
    const float ldt = bf2f(a.logdt[ch]);
    const u32x4 xrow = *(const u32x4*)(a.x + (size_t)(8 * b + w) * D + 8 * lane);
    const u32x4 xtile = *(const u32x4*)(a.x + (size_t)(64 * (b >> 3) + (tid >> 3)) * D + 64 * (b & 7) + 8 * (tid & 7));
    const bool isq = b < 192;
    const int id2 = isq ? b : b - 192;
    const int kq = id2 & 3, nb = id2 >> 2;
    const u32x4 wv = *(const u32x4*)((isq ? a.wqkv : a.wo) + (size_t)(128 * kq + (tid >> 2)) * (isq ? 1536 : 512) + 32 * nb + 8 * (tid & 3));
    const __amdgpu_buffer_rsrc_t rsp = __builtin_amdgcn_make_buffer_rsrc((void*)ws, 0, WS_USED, 0x00020000);
#define PSTORE(v, o) ST128(v, rsp, o)
#define PZ 0
    const u32x4 pz = {POISON, POISON, POISON, POISON};
#define POIS(k) ST128(pz, rsp, ((k) == 0 ? OFF_XT1 + (unsigned)(b * 512 + tid) * 16u : (k) == 1 ? OFF_XT2 + (unsigned)(b * 512 + tid) * 16u : \
    (k) == 2 ? OFF_KV + (unsigned)(b * 1024 + tid) * 16u : (k) == 3 ? OFF_KV + (unsigned)(b * 1024 + 512 + tid) * 16u : OFF_OF + (unsigned)(b * 512 + tid) * 16u))
    {
        u32x4 z = {0u, 0u, 0u, 0u};
        if (tid < 8) PSTORE(z, OFF_ACC + (unsigned)(b * 8 + tid) * 16u);
        POIS(0); POIS(1); POIS(2); POIS(3); POIS(4);
    }
    {
        *(u32x4*)(smem + P_X + (tid >> 3) * 144 + 16 * (tid & 7)) = xtile;
        *(u32x4*)(smem + P_W + (tid >> 2) * 80 + 16 * (tid & 3)) = wv;
        float s = 0.f;
#pragma unroll
        for (int j = 0; j < 4; ++j) s = dot2(xrow[j], 0x3f803f80u, s);
        s = xrow16_sum(row16_sum(s));
        const float mean = s * (1.f / 512.f);
        float q = 0.f;
#pragma unroll
        for (int j = 0; j < 4; ++j) { float d0 = bflo(xrow[j]) - mean, d1 = bfhi(xrow[j]) - mean; q += d0 * d0 + d1 * d1; }
        q = xrow16_sum(row16_sum(q));
        if (lane == 0) ((f2_t*)(ws + OFF_ST0))[8 * b + w] = ln_scalars(mean, q * (1.f / 512.f));
    }
    __syncthreads();
    {
        const int q = l15 >> 2, p = lane & 3;
        {
            const int d0 = 16 * (w & 3), t0 = 32 * (w >> 2) + 8 * g;
            const lds_char* src = (const lds_char*)smem + P_X + (t0 + q) * 144 + (d0 + 4 * p) * 2;
            u32x2 r0 = __builtin_bit_cast(u32x2, TRREAD(src)), r1 = __builtin_bit_cast(u32x2, TRREAD(src + 4 * 144));
            PSTORE((u32x4{r0[0], r0[1], r1[0], r1[1]}), OFF_XT0 + (unsigned)(64 * (b & 7) + d0 + l15) * (T * 2) + 2 * (64 * (b >> 3) + t0));
        }
        {
            const int nt2 = w & 1, k4 = w >> 1;
            const lds_char* src = (const lds_char*)smem + P_W + (32 * k4 + 8 * g + q) * 80 + (16 * nt2 + 4 * p) * 2;
            u32x2 r0 = __builtin_bit_cast(u32x2, TRREAD(src)), r1 = __builtin_bit_cast(u32x2, TRREAD(src + 4 * 80));
            u32x4 ov = {r0[0], r0[1], r1[0], r1[1]};
            const int n16 = 2 * nb + nt2, ks = 4 * kq + k4;
            size_t off;
            if (isq) off = OFF_WF + ((size_t)(((n16 & 31) * 3 + (n16 >> 5)) * 16 + ks) * 64 + lane) * 16;
            else off = OFF_WOF + ((size_t)(n16 * 16 + ks) * 64 + lane) * 16;
            PSTORE(ov, (unsigned)off);
            float cs = 0.f;
#pragma unroll
            for (int j = 0; j < 4; ++j) cs = dot2(ov[j], 0x3f803f80u, cs);
            cs = xrow16_sum(cs);
            if (g == 0) ((float*)(smem + P_CS))[w * 16 + l15] = cs;
        }
    }
    {
        const float dt = rbf(expf(ldt));
        PST(1);
        const float lo = (float)(8 * l15);
        float hv[3];
#pragma unroll
        for (int mt = 0; mt < 3; ++mt) hv[mt] = hival(16 * mt + l15);
        f32x4 acc[3];
#pragma unroll
        for (int mt = 0; mt < 3; ++mt) acc[mt] = f32x4{0.f, 0.f, 0.f, 0.f};
#pragma unroll
        for (int ks = 0; ks < 2; ++ks) {
            u32x4 Bh, Bl, Ah[3];
#pragma unroll
            for (int i = 0; i < 4; ++i) {
                const int wd = i >> 1;
                const bool hi = i & 1;
                float Ar = hi ? bfhi(vAr[ks][wd]) : bflo(vAr[ks][wd]), Ai = hi ? bfhi(vAi[ks][wd]) : bflo(vAi[ks][wd]);
                float Br = hi ? bfhi(vBr[ks][wd]) : bflo(vBr[ks][wd]), Bi = hi ? bfhi(vBi[ks][wd]) : bflo(vBi[ks][wd]);
                float Cr = hi ? bfhi(vCr[ks][wd]) : bflo(vCr[ks][wd]), Ci = hi ? bfhi(vCi[ks][wd]) : bflo(vCi[ks][wd]);
                const f2_t ef = {(dt * Ar) * 1.4426950408889634f, (dt * Ai) * 0.15915494309189535f};
                float dBr = dt * Br, dBi = dt * Bi;
                float cbr = Cr * dBr - Ci * dBi, cbi = Cr * dBi + Ci * dBr;
                f2_t e0 = cexp_l(ef, lo);
                f2_t gg = {cbr * e0[0] - cbi * e0[1], -(cbr * e0[1] + cbi * e0[0])};
                unsigned hh, ll;
                split2(gg, hh, ll);
                Bh[i] = hh; Bl[i] = ll;
#pragma unroll
                for (int mt = 0; mt < 3; ++mt) {
                    const f2_t ea = cexp_l(ef, hv[mt]);
                    Ah[mt][i] = pk(ea[0], ea[1]);
                }
            }
            bf16x8 bh = __builtin_bit_cast(bf16x8, Bh), bl = __builtin_bit_cast(bf16x8, Bl);
#pragma unroll
            for (int mt = 0; mt < 3; ++mt) {
                bf16x8 ah = __builtin_bit_cast(bf16x8, Ah[mt]);
                acc[mt] = MFMA16(ah, bl, acc[mt]);
                acc[mt] = MFMA16(ah, bh, acc[mt]);
            }
        }
        PST(2);
        float* kw = (float*)smem + w * 648;
        const int p0 = 4 * (g & 1) + 128 * (g >> 1) + 8 * l15;
        *(f32x4*)(kw + p0) = acc[0];
        if (g < 2) *(f32x4*)(kw + 256 + 64 * g + 4 * l15) = acc[1];
        else {
            const int pb = 384 + 64 * (g - 2) + 2 * l15;
            *(f2_t*)(kw + pb) = f2_t{acc[1][0], acc[1][1]};
            *(f2_t*)(kw + pb + 32) = f2_t{acc[1][2], acc[1][3]};
        }
        if (g < 2) {
#pragma unroll
            for (int r = 0; r < 4; ++r) kw[512 + 16 * (4 * g + r) + l15] = acc[2][r];
        } else if (lane == 32) kw[640] = acc[2][0];
    }
    __syncthreads();
    PST(3);
    {
        const float* kq_ = (const float*)smem;
        float* kt = (float*)smem + 8 * 648;
        if (tid < 2 * 162) {
            const int c = tid >= 162, pi = tid - 162 * c;
            const f32x4* kp = (const f32x4*)(kq_ + c * (4 * 648)) + pi;
            ((f32x4*)kt)[tid] = (kp[0] + kp[162]) + (kp[324] + kp[486]);
        }
        if (isq && tid >= 480) {
            const float* cp = (const float*)(smem + P_CS) + (tid - 480);
            ((float*)(ws + OFF_CS))[kq * 1536 + 32 * nb + (tid - 480)] = (cp[0] + cp[32]) + (cp[64] + cp[96]);
        }
        __syncthreads();
        if (tid < 8) PSTORE((u32x4{0u, 0u, 0u, 0u}), OFF_R + (unsigned)(2 * b + (tid >> 2)) * 4160 + 16 * (256 + (tid & 3)));
        {
            const int c = tid >> 8, ci = tid & 255;
            const int m = 255 - ci;
            int base; unsigned offs;
            if (m < 32) { base = 8 * m; offs = 0x76543210u; }
            else if (m < 64) { base = 256 + 4 * (m - 32); offs = 0x43222100u; }
            else if (m < 128) { base = 384 + 2 * (m - 64); offs = 0x22111000u; }
            else { base = 512 + (m - 128); offs = 0x11100000u | ((unsigned)(m & 1) << 16); }
            float v[8];
#pragma unroll
            for (int j = 0; j < 8; ++j) {
                const int i = 7 - j;
                v[j] = kt[c * 648 + base + ((offs >> (4 * i)) & 15)];
            }
            u32x4 o = {pk(v[0], v[1]), pk(v[2], v[3]), pk(v[4], v[5]), pk(v[6], v[7])};
            PSTORE(o, OFF_R + (unsigned)(2 * b + c) * 4160 + 16 * ci);
        }
    }
    PST(4);
}

constexpr int CV_RC = 4160, CV_RCH = 8 * CV_RC;
constexpr int CV_U0 = 2 * CV_RCH, CV_UPL = 1536, CV_UCH = 4 * CV_UPL;
constexpr int CV_Y0 = CV_U0 + 2 * CV_UCH;
constexpr int CV_YP = 64 * 144;
constexpr int Q_PART = 65536;
constexpr int A_KV = Q_PART + 2048;
constexpr int A_ONES = A_KV + 4 * 20480;
constexpr int MEGA_LDS = CV_Y0 + 8 * CV_YP;
static_assert(MEGA_LDS <= 163840 && A_ONES + 1024 <= MEGA_LDS, "lds");

struct MegaArgs {
    const u16* Dskip;
    u16* out;
    char* ws;
};

constexpr unsigned SPIN_MAX = 2000000u;
DEV u32x4 sanit(u32x4 v) {
#pragma unroll
    for (int j = 0; j < 4; ++j) v[j] = min(v[j], 0xFFFFFFFEu);
    return v;
}
DEV bool has_poison(u32x4 v) { return max(max(v[0], v[1]), max(v[2], v[3])) == POISON; }
#define DMA_VALID(N, SRC, DST, LIVE) { \
    unsigned todo_ = (LIVE); \
    VM0(); \
    for (unsigned spins_ = 0; todo_ != 0u && spins_ < SPIN_MAX; ++spins_) { \
        if (spins_) __builtin_amdgcn_s_sleep(4); \
        _Pragma("unroll") for (int i = 0; i < (N); ++i) \
            if (todo_ & (1u << i)) __builtin_amdgcn_global_load_lds((const unsigned*)(SRC(i)), (lds_u32*)(DST(i)), 16, 0, SC1); \
        VM0(); \
        u32x4 v_[N]; \
        _Pragma("unroll") for (int i = 0; i < (N); ++i) v_[i] = *(const u32x4*)((const char*)(DST(i)) + lane * 16); \
        unsigned bad_ = 0u; \
        _Pragma("unroll") for (int i = 0; i < (N); ++i) bad_ |= __any(has_poison(v_[i])) ? (1u << i) : 0u; \
        todo_ &= bad_; \
    } }
#define DMA_ISSUE(N, SRC, DST, LIVE) { \
    _Pragma("unroll") for (int i = 0; i < (N); ++i) \
        if ((LIVE) & (1u << i)) __builtin_amdgcn_global_load_lds((const unsigned*)(SRC(i)), (lds_u32*)(DST(i)), 16, 0, SC1); }
#define DMA_VALID2(N, SRC, DST, LIVE) { \
    unsigned todo_ = (LIVE); \
    for (unsigned spins_ = 0; todo_ != 0u && spins_ < SPIN_MAX; ++spins_) { \
        if (spins_ > 1) __builtin_amdgcn_s_sleep(4); \
        if (spins_) { \
            _Pragma("unroll") for (int i = 0; i < (N); ++i) \
                if (todo_ & (1u << i)) __builtin_amdgcn_global_load_lds((const unsigned*)(SRC(i)), (lds_u32*)(DST(i)), 16, 0, SC1); \
        } \
        VM0(); \
        u32x4 v_[N]; \
        _Pragma("unroll") for (int i = 0; i < (N); ++i) v_[i] = *(const u32x4*)((const char*)(DST(i)) + lane * 16); \
        unsigned bad_ = 0u; \
        _Pragma("unroll") for (int i = 0; i < (N); ++i) bad_ |= __any(has_poison(v_[i])) ? (1u << i) : 0u; \
        todo_ &= bad_; \
    } \
    VM0(); }
DEV float max3f(float a, float b, float c) { return __builtin_fmaxf(__builtin_fmaxf(a, b), c); }
DEV float max2f(float a, float b) { return __builtin_fmaxf(a, b); }
template <bool EDGE>
DEV u32x4 attn_body(const char* smem, int hw, int kh, int lane, int ab, const bf16x8 Qf, const bf16x8* wo, bf16x8 (&Wo)[16]) {
    const int r32 = lane & 31, hh = lane >> 5;
    const float NEG = -1.0e30f;
    const char* kb = smem + A_KV + hw * 20480 + kh * 2048 + lane * 16;
    const char* vb = (r32 < 16 ? smem + A_KV + hw * 20480 + kh * 2048 + 1024 : smem + A_ONES) + hh * 256 + (r32 & 15) * 16;
    const int vstep = r32 < 16 ? 2048 : 0;
    bf16x8 Kf[9];
#pragma unroll
    for (int j = 0; j < 9; ++j) Kf[j] = *(const bf16x8*)(kb + j * 2048);
    f32x16 zero;
#pragma unroll
    for (int i = 0; i < 16; ++i) zero[i] = 0.f;
    float m0 = NEG, m1 = NEG;
    {
        f32x16 Sn = MFMA32(Kf[0], Qf, zero);
#pragma unroll
        for (int j = 0; j < 9; ++j) {
            f32x16 S = Sn;
            if (j < 8) Sn = MFMA32(Kf[j + 1], Qf, zero);
            SB();
            if (j == 0) {
#pragma unroll
                for (int i = 0; i < 16; ++i) if (!((i & 3) + 8 * (i >> 2) + 4 * hh > r32)) S[i] = NEG;
            }
            if (j == 8) {
#pragma unroll
                for (int i = 0; i < 16; ++i) if ((i & 3) + 8 * (i >> 2) + 4 * hh > r32) S[i] = NEG;
            }
            float a0 = m0, a1 = m1;
#pragma unroll
            for (int i = 0; i < 16; i += 4) { a0 = max3f(a0, S[i], S[i + 1]); a1 = max3f(a1, S[i + 2], S[i + 3]); }
            if (EDGE) { if (ab - 8 + j >= 0) { m0 = a0; m1 = a1; } }
            else { m0 = a0; m1 = a1; }
            SB();
        }
    }
    float m = max2f(m0, m1);
    {
        auto t = __builtin_amdgcn_permlane32_swap(__float_as_uint(m), __float_as_uint(m), false, false);
        m = max2f(__uint_as_float(t[0]), __uint_as_float(t[1]));
    }
    f32x16 nm;
#pragma unroll
    for (int i = 0; i < 16; ++i) nm[i] = -m;
    f32x16 O = zero;
    {
        f32x16 Sn = MFMA32(Kf[0], Qf, nm);
        bf16x8 vn0 = *(const bf16x8*)(vb), vn1 = *(const bf16x8*)(vb + 512);
#pragma unroll
        for (int j = 0; j < 9; ++j) {
            f32x16 S = Sn;
            const bf16x8 v0 = vn0, v1 = vn1;
            if (j < 8) {
                Sn = MFMA32(Kf[j + 1], Qf, nm);
                vn0 = *(const bf16x8*)(vb + (j + 1) * vstep); vn1 = *(const bf16x8*)(vb + (j + 1) * vstep + 512);
                Wo[j] = wo[j * 64];
            }
            SB();
            if (j == 0) {
#pragma unroll
                for (int i = 0; i < 16; ++i) if (!((i & 3) + 8 * (i >> 2) + 4 * hh > r32)) S[i] = NEG;
            }
            if (j == 8) {
#pragma unroll
                for (int i = 0; i < 16; ++i) if ((i & 3) + 8 * (i >> 2) + 4 * hh > r32) S[i] = NEG;
            }
            u32x4 p0, p1;
#pragma unroll
            for (int i = 0; i < 4; ++i) p0[i] = pk(__builtin_amdgcn_exp2f(S[2 * i]), __builtin_amdgcn_exp2f(S[2 * i + 1]));
            if (!EDGE || ab - 8 + j >= 0) O = MFMA32(v0, __builtin_bit_cast(bf16x8, p0), O);
#pragma unroll
            for (int i = 0; i < 4; ++i) p1[i] = pk(__builtin_amdgcn_exp2f(S[8 + 2 * i]), __builtin_amdgcn_exp2f(S[8 + 2 * i + 1]));
            if (!EDGE || ab - 8 + j >= 0) O = MFMA32(v1, __builtin_bit_cast(bf16x8, p1), O);
            SB();
        }
    }
    const float inv = __builtin_amdgcn_rcpf(O[8]);
    const unsigned a0 = pk(O[0] * inv, O[1] * inv), a1 = pk(O[2] * inv, O[3] * inv);
    const unsigned b0 = pk(O[4] * inv, O[5] * inv), b1 = pk(O[6] * inv, O[7] * inv);
    auto sx = __builtin_amdgcn_permlane32_swap(a0, b0, false, false);
    auto sy = __builtin_amdgcn_permlane32_swap(a1, b1, false, false);
    return u32x4{sx[0], sy[0], sx[1], sy[1]};
}

DEV u32x4 attn_fast(const char* smem, int hw, int kh, int lane, const bf16x8 Qf, const bf16x8* wo, bf16x8 (&Wo)[16]) {
    const int r32 = lane & 31, hh = lane >> 5;
    const char* kb = smem + A_KV + hw * 20480 + kh * 2048 + lane * 16;
    const char* vb = (r32 < 16 ? smem + A_KV + hw * 20480 + kh * 2048 + 1024 : smem + A_ONES) + hh * 256 + (r32 & 15) * 16;
    const int vstep = r32 < 16 ? 2048 : 0;
    bf16x8 Kf[9];
#pragma unroll
    for (int j = 0; j < 9; ++j) Kf[j] = *(const bf16x8*)(kb + j * 2048);
    f32x16 zero;
#pragma unroll
    for (int i = 0; i < 16; ++i) zero[i] = 0.f;
    float m0, m1;
    {
        f32x16 Sa = MFMA32(Kf[0], Qf, zero), Sb = MFMA32(Kf[8], Qf, zero);
        f32x16 Sn = MFMA32(Kf[1], Qf, zero);
        SB();
#pragma unroll
        for (int i = 0; i < 16; ++i) Sa[i] = ((i & 3) + 8 * (i >> 2) + 4 * hh > r32) ? Sa[i] : Sb[i];
        m0 = max3f(Sa[0], Sa[1], Sa[2]); m1 = max3f(Sa[3], Sa[4], Sa[5]);
        m0 = max3f(m0, Sa[6], Sa[7]);
#pragma unroll
        for (int i = 8; i < 16; i += 4) { m0 = max3f(m0, Sa[i], Sa[i + 1]); m1 = max3f(m1, Sa[i + 2], Sa[i + 3]); }
        SB();
#pragma unroll
        for (int j = 1; j < 8; ++j) {
            f32x16 S = Sn;
            if (j < 7) Sn = MFMA32(Kf[j + 1], Qf, zero);
            SB();
#pragma unroll
            for (int i = 0; i < 16; i += 4) { m0 = max3f(m0, S[i], S[i + 1]); m1 = max3f(m1, S[i + 2], S[i + 3]); }
            SB();
        }
    }
    float m = max2f(m0, m1);
    {
        auto t = __builtin_amdgcn_permlane32_swap(__float_as_uint(m), __float_as_uint(m), false, false);
        m = max2f(__uint_as_float(t[0]), __uint_as_float(t[1]));
    }
    f32x16 nm;
#pragma unroll
    for (int i = 0; i < 16; ++i) nm[i] = -m;
    f32x16 O = zero;
    {
        f32x16 Sn = MFMA32(Kf[1], Qf, nm);
        bf16x8 vn0 = *(const bf16x8*)(vb + vstep), vn1 = *(const bf16x8*)(vb + vstep + 512);
        f32x16 Sb;
        bf16x8 vb0, vb1;
#pragma unroll
        for (int j = 1; j < 8; ++j) {
            f32x16 S = Sn;
            const bf16x8 v0 = vn0, v1 = vn1;
            const int jn = j < 7 ? j + 1 : 0;
            Sn = MFMA32(Kf[jn], Qf, nm);
            vn0 = *(const bf16x8*)(vb + jn * vstep); vn1 = *(const bf16x8*)(vb + jn * vstep + 512);
            if (j == 7) { Sb = MFMA32(Kf[8], Qf, nm); vb0 = *(const bf16x8*)(vb + 8 * vstep); vb1 = *(const bf16x8*)(vb + 8 * vstep + 512); }
            Wo[j - 1] = wo[(j - 1) * 64];
            if (j == 7) Wo[7] = wo[7 * 64];
            SB();
            u32x4 p0, p1;
#pragma unroll
            for (int i = 0; i < 4; ++i) p0[i] = pk(__builtin_amdgcn_exp2f(S[2 * i]), __builtin_amdgcn_exp2f(S[2 * i + 1]));
            O = MFMA32(v0, __builtin_bit_cast(bf16x8, p0), O);
#pragma unroll
            for (int i = 0; i < 4; ++i) p1[i] = pk(__builtin_amdgcn_exp2f(S[8 + 2 * i]), __builtin_amdgcn_exp2f(S[8 + 2 * i + 1]));
            O = MFMA32(v1, __builtin_bit_cast(bf16x8, p1), O);
            SB();
        }
        u32x4 pa[2], pb[2];
#pragma unroll
        for (int i = 0; i < 8; ++i) {
            const int k0 = ((2 * i) & 3) + 8 * ((2 * i) >> 2) + 4 * hh;
            const bool f0 = k0 > r32, f1 = k0 + 1 > r32;
            const unsigned pp = pk(__builtin_amdgcn_exp2f(f0 ? Sn[2 * i] : Sb[2 * i]), __builtin_amdgcn_exp2f(f1 ? Sn[2 * i + 1] : Sb[2 * i + 1]));
            const unsigned msk = (f0 ? 0xffffu : 0u) | (f1 ? 0xffff0000u : 0u);
            pa[i >> 2][i & 3] = pp & msk;
            pb[i >> 2][i & 3] = pp & ~msk;
        }
        O = MFMA32(vn0, __builtin_bit_cast(bf16x8, pa[0]), O);
        O = MFMA32(vn1, __builtin_bit_cast(bf16x8, pa[1]), O);
        O = MFMA32(vb0, __builtin_bit_cast(bf16x8, pb[0]), O);
        O = MFMA32(vb1, __builtin_bit_cast(bf16x8, pb[1]), O);
    }
    const float inv = __builtin_amdgcn_rcpf(O[8]);
    const unsigned a0 = pk(O[0] * inv, O[1] * inv), a1 = pk(O[2] * inv, O[3] * inv);
    const unsigned b0 = pk(O[4] * inv, O[5] * inv), b1 = pk(O[6] * inv, O[7] * inv);
    auto sx = __builtin_amdgcn_permlane32_swap(a0, b0, false, false);
    auto sy = __builtin_amdgcn_permlane32_swap(a1, b1, false, false);
    return u32x4{sx[0], sy[0], sx[1], sy[1]};
}

#define QD 16
#define OEARLY 0
#define NE (layer == 0 ? 0 : 8)
#define RLOAD() { const int ci2_ = 256 + (tid & 3), c2_ = (tid >> 2) & 1; \
    const u32x4* src_ = (const u32x4*)(ws + OFF_R + (size_t)(2 * b + (tid >> 8)) * 4160 + 16 * (tid & 255)); \
    const u32x4* src2_ = (const u32x4*)(ws + OFF_R + (size_t)(2 * b + c2_) * 4160 + 16 * (ci2_ > 258 ? 258 : ci2_)); \
    Rv[0] = src_[0]; Rv[1] = src_[1]; Rv[2] = src2_[0]; Rv[3] = src2_[1]; }
#define WLOAD(id_) { const int i_ = (id_) >> 1; \
    if ((id_) & 1) Wv[i_] = wf[(32 + ((i_ + 4 * hw) & 15)) * 64]; \
    else Wk[i_] = wf[(16 + ((i_ + 4 * hw) & 15)) * 64]; }
template <int layer>
DEV void layer_body(const MegaArgs& a, char* smem, u32x4 (&Rv)[4]) {
    char* ws = a.ws;
    const int tid0 = threadIdx.x;
    const int w = __builtin_amdgcn_readfirstlane(tid0 >> 6);
    const int b = blockIdx.x;
    const int tt = 4 * (b & 7) + ((b >> 3) & 3), hg = b >> 5;
    const int hw = w & 3, kh = w >> 2;
    const int h = 4 * hg + hw;
    const __amdgpu_buffer_rsrc_t rs = __builtin_amdgcn_make_buffer_rsrc((void*)ws, 0, WS_USED, 0x00020000);
    {
        const unsigned xin = layer == 0 ? OFF_XT0 : OFF_XT2;
        const unsigned xmid = layer == 0 ? OFF_XT1 : OFF_XT3;
        const unsigned kvb = OFF_KV + layer * 4 * MB;
        const unsigned ofb = OFF_OF + layer * 2 * MB;
        bf16x8 Wk[16], Wv[16];
        f32x4 csq = {0.f, 0.f, 0.f, 0.f}, csk = {0.f, 0.f, 0.f, 0.f};
        float csv = 0.f;
        {
            PHASE_IDS();
            const int c = tid >> 8, ti = tid & 255;
            u32x4 xv, sv[4], s0v[2], pf[8];
            if (layer == 0) {
                xv = *(const u32x4*)(ws + xin + (size_t)(2 * b + c) * (T * 2) + 16 * ti);
                s0v[0] = *(const u32x4*)(ws + OFF_ST0 + 32 * tid);
                s0v[1] = *(const u32x4*)(ws + OFF_ST0 + 32 * tid + 16);
            }
            const float dk = bf2f(a.Dskip[2 * b + c]);
            {
                const int ci2 = 256 + (tid & 3), c2 = (tid >> 2) & 1;
                if (layer == 0) { RLOAD(); SB(); }
                const u32x4 v0 = Rv[0], v1 = Rv[1], z0 = Rv[2], z1 = Rv[3];
#pragma unroll
                for (int rep = 0; rep < 2; ++rep) {
                    if (rep == 1 && tid >= 8) break;
                    unsigned dw[8];
#pragma unroll
                    for (int j = 0; j < 4; ++j) { dw[j] = rep ? z0[j] : v0[j]; dw[4 + j] = rep ? z1[j] : v1[j]; }
                    char* dst = smem + (rep ? c2 * CV_RCH + 16 * (ci2 > 258 ? 258 : ci2) : c * CV_RCH + 16 * ti);
#pragma unroll
                    for (int s = 0; s < 8; ++s) {
                        u32x4 o;
#pragma unroll
                        for (int d = 0; d < 4; ++d) {
                            const int k = (s >> 1) + d;
                            o[d] = (s & 1) ? __builtin_amdgcn_alignbyte(dw[k + 1], dw[k], 2) : dw[k];
                        }
                        *(u32x4*)(dst + s * CV_RC) = o;
                    }
                }
                if (tid < 256) *(u32x4*)(smem + CV_U0 + (tid >> 5) * CV_UPL + 16 * (tid & 31)) = u32x4{0u, 0u, 0u, 0u};
            }
            STAMP(16 * layer + 1);
            const bf16x8* wf = (const bf16x8*)(ws + OFF_WF) + (size_t)(h * 3) * 16 * 64 + lane;
            f32x4* stl = (f32x4*)(smem + CV_Y0);
            if (layer != 0) {
                u32x4 av[4];
                VM0();
                bool bad = true;
                for (unsigned spins = 0; spins < SPIN_MAX; ++spins) {
                    asm volatile("" ::: "memory");
                    if (bad) {
                        xv = LD128(rs, xin + (unsigned)(2 * b + c) * (T * 2) + 16 * ti);
                        bad = has_poison(xv);
                    }
                    if (!__any(bad)) break;
                    __builtin_amdgcn_s_sleep(4);
                }
                BAR();
                bad = true;
                for (unsigned spins = 0; spins < SPIN_MAX; ++spins) {
                    asm volatile("" ::: "memory");
                    if (bad) {
#pragma unroll
                        for (int j = 0; j < 4; ++j) av[j] = LD128(rs, OFF_ACC + 8192 * j + 16 * tid);
                        bad = false;
#pragma unroll
                        for (int j = 0; j < 4; ++j) bad |= (av[j][1] >> 26) != 32u || (av[j][3] >> 26) != 32u;
                    }
                    if (!__any(bad)) break;
                    __builtin_amdgcn_s_sleep(8);
                }
                STAMP(16 + 2);
#pragma unroll
                for (int j = 0; j < 4; ++j) {
#pragma unroll
                    for (int id = j * NE / 4; id < (j + 1) * NE / 4; ++id) WLOAD(id);
                    SB();
                    const float h1 = (float)(int)((av[j][1] & 0x03FFFFFFu) - 0x01000000u), h2 = (float)(int)((av[j][3] & 0x03FFFFFFu) - 0x01000000u);
                    const float m1 = (float)(av[j][0] >> 16), m2 = (float)(av[j][2] >> 16);
                    const float l1 = (float)(av[j][0] & 0xFFFFu), l2 = (float)(av[j][2] & 0xFFFFu);
                    const float mean = __builtin_fmaf(__builtin_fmaf(h1, 65536.f, m1), 65536.f, l1) * (1.f / (1073741824.f * 512.f));
                    const float ex2 = __builtin_fmaf(__builtin_fmaf(h2, 65536.f, m2), 65536.f, l2) * (1.f / (16777216.f * 512.f));
                    ((f2_t*)stl)[512 * j + tid] = ln_scalars(mean, fmaxf(__builtin_fmaf(-mean, mean, ex2), 0.f));
                }
            } else {
                stl[2 * tid] = __builtin_bit_cast(f32x4, s0v[0]);
                stl[2 * tid + 1] = __builtin_bit_cast(f32x4, s0v[1]);
                SB();
#pragma unroll
                for (int id = 0; id < NE; ++id) WLOAD(id);
                SB();
            }
            BAR();
#pragma unroll
            for (int j = 0; j < 4; ++j) sv[j] = __builtin_bit_cast(u32x4, stl[4 * ti + j]);
            u32x4 uv;
#pragma unroll
            for (int j = 0; j < 4; ++j) {
                float u0 = rbf(bflo(xv[j]) - __uint_as_float(sv[j][0])) * __uint_as_float(sv[j][1]);
                float u1 = rbf(bfhi(xv[j]) - __uint_as_float(sv[j][2])) * __uint_as_float(sv[j][3]);
                uv[j] = pk(u0, u1);
            }
            *(u32x4*)(smem + CV_U0 + c * CV_UCH + (ti & 3) * CV_UPL + 16 * (32 + (ti >> 2))) = uv;
            BAR();
            STAMP(16 * layer + 3);
            const float* cs = (const float*)(ws + OFF_CS);
            f32x4 cq[4], ck[4];
            float cv[4];
            {
                const int cw = w >> 2, kk = w & 1, par = (w >> 1) & 1;
                const int r = lane & 31, hh = lane >> 5;
                const int rho = r >> 3, cc = r & 7;
                const char* pa = smem + cw * CV_RCH + (7 - cc) * CV_RC + 16 * (255 + 2 * kk - rho + hh) - 64 * par;
                const char* pb = smem + CV_U0 + cw * CV_UCH + (2 * kk + hh) * CV_UPL + 16 * (32 + r) - 16 * par;
                f32x16 acc0, acc1;
#pragma unroll
                for (int i = 0; i < 16; ++i) { acc0[i] = 0.f; acc1[i] = 0.f; }
                bf16x8 a0 = *(const bf16x8*)(pa), a1 = *(const bf16x8*)(pa - 2048);
                bf16x8 b0 = *(const bf16x8*)(pb), b1 = *(const bf16x8*)(pb + 512);
#pragma unroll
                for (int it = 0; it < 16; ++it) {
                    bf16x8 na0, na1, nb0, nb1;
                    if (it < 15) {
                        na0 = *(const bf16x8*)(pa - 128 * (it + 1));
                        na1 = *(const bf16x8*)(pa - 128 * (it + 1) - 2048);
                        nb0 = *(const bf16x8*)(pb - 32 * (it + 1));
                        nb1 = *(const bf16x8*)(pb - 32 * (it + 1) + 512);
                    }
#pragma unroll
                    for (int id = NE + it * (32 - NE) / 16; id < NE + (it + 1) * (32 - NE) / 16; ++id) WLOAD(id);
                    SB();
                    acc0 = MFMA32(a0, b0, acc0);
                    acc1 = MFMA32(a0, b1, acc1);
                    acc1 = MFMA32(a1, b0, acc1);
                    SB();
                    if (it < 15) { a0 = na0; a1 = na1; b0 = nb0; b1 = nb1; }
                }
                char* yp = smem + CV_Y0 + w * CV_YP + r * 144 + 16 * hh;
#pragma unroll
                for (int q = 0; q < 4; ++q) {
                    f32x4 v0 = {acc0[4 * q], acc0[4 * q + 1], acc0[4 * q + 2], acc0[4 * q + 3]};
                    f32x4 v1 = {acc1[4 * q], acc1[4 * q + 1], acc1[4 * q + 2], acc1[4 * q + 3]};
                    *(f32x4*)(yp + 32 * q) = v0;
                    *(f32x4*)(yp + 32 * 144 + 32 * q) = v1;
                }
                SB();
#pragma unroll
                for (int kq = 0; kq < 4; ++kq) {
                    cq[kq] = *(const f32x4*)(cs + kq * 1536 + h * 16 + 4 * g);
                    ck[kq] = *(const f32x4*)(cs + kq * 1536 + 512 + h * 16 + 4 * g);
                    cv[kq] = cs[kq * 1536 + 1024 + h * 16 + l15];
                }
                SB();
            }
            STAMP(16 * layer + 4);
            BAR();
            {
                const char* y0 = smem + CV_Y0 + (4 * c) * CV_YP + (ti >> 2) * 144 + (ti & 3) * 32;
                f32x4 pp[8];
#pragma unroll
                for (int p = 0; p < 4; ++p) { pp[2 * p] = *(const f32x4*)(y0 + p * CV_YP); pp[2 * p + 1] = *(const f32x4*)(y0 + p * CV_YP + 16); }
                f32x4 ya = (pp[0] + pp[2]) + (pp[4] + pp[6]), yb = (pp[1] + pp[3]) + (pp[5] + pp[7]);
                float y[8] = {ya[0], ya[1], ya[2], ya[3], yb[0], yb[1], yb[2], yb[3]};
                u32x4 o;
#pragma unroll
                for (int j = 0; j < 4; ++j) {
                    float s0 = rbf(rbf(y[2 * j]) + rbf(bflo(uv[j]) * dk));
                    float s1 = rbf(rbf(y[2 * j + 1]) + rbf(bfhi(uv[j]) * dk));
                    o[j] = pk(bflo(xv[j]) + s0, bfhi(xv[j]) + s1);
                }
                ST128(sanit(o), rs, xmid + (unsigned)(2 * b + c) * (T * 2) + 16 * ti);
                SB();
                csq = (cq[0] + cq[1]) + (cq[2] + cq[3]);
                csk = (ck[0] + ck[1]) + (ck[2] + ck[3]);
                csv = (cv[0] + cv[1]) + (cv[2] + cv[3]);
            }
            BAR();
            STAMP(16 * layer + 5);
        }
        u32x2 xres[2];
        bf16x8 Qf;
        const int P0 = 2 * tt - 8 + 4 * kh;
        const unsigned live = P0 >= 0 ? 0xFFu : (P0 <= -4 ? 0u : (0xFFu << (2 * (-P0))) & 0xFFu);
        {
            PHASE_IDS();
            {
                const int rr = lane >> 3, chp = lane & 7;
                const int f = ((rr >> 1) & 1) | ((w & 1) << 1);
                const char* src0 = ws + xmid + (size_t)(8 * w + rr) * (T * 2) + 128 * tt + 16 * (chp ^ (2 * f));
#define XSRC(i) (src0 + (size_t)(i) * (64 * T * 2))
#define XDST(i) (smem + 1024 * (w + 8 * (i)))
                DMA_VALID(8, XSRC, XDST, 0xFFu);
            }
            BAR();
            STAMP(16 * layer + 7);
            {
                const int d = 64 * hg + 16 * hw + l15;
                const int f = ((d >> 1) & 1) | (((d >> 3) & 1) << 1);
#pragma unroll
                for (int i = 0; i < 2; ++i)
                    xres[i] = *(const u32x2*)(smem + d * 128 + 16 * ((4 * kh + 2 * i + (g >> 1)) ^ (2 * f)) + 8 * (g & 1));
            }
            const int q = l15 >> 2, p = lane & 3;
            const int f = ((q >> 1) & 1) | ((g & 1) << 1);
            const lds_char* lbase = (const lds_char*)smem + (8 * g + q) * 128 + 8 * (p & 1);
            const bf16x8* wq = (const bf16x8*)(ws + OFF_WF) + (size_t)(h * 3) * 16 * 64 + lane;
            bf16x8 Wq[16];
#define TRLD(dst, i_) { const lds_char* lb_ = lbase + (((i_) + 4 * hw) & 15) * 4096; _Pragma("unroll") for (int ti = 0; ti < 2; ++ti) { \
                const int ch = (2 * (2 * kh + ti) + (p >> 1)) ^ (2 * f); \
                v4s x0 = TRREAD(lb_ + 16 * ch); \
                v4s x1 = TRREAD(lb_ + 512 + 16 * ch); \
                dst[ti] = bf16x8{x0[0], x0[1], x0[2], x0[3], x1[0], x1[1], x1[2], x1[3]}; } }
            f32x4 ak[2], av[2];
            f32x4 d1[2] = {f32x4{0.f, 0.f, 0.f, 0.f}, f32x4{0.f, 0.f, 0.f, 0.f}}, d2[2] = {f32x4{0.f, 0.f, 0.f, 0.f}, f32x4{0.f, 0.f, 0.f, 0.f}};
            const bf16x8 ones = {0x3f80, 0x3f80, 0x3f80, 0x3f80, 0x3f80, 0x3f80, 0x3f80, 0x3f80};
#pragma unroll
            for (int i = 0; i < 2; ++i) { ak[i] = f32x4{0.f, 0.f, 0.f, 0.f}; av[i] = f32x4{0.f, 0.f, 0.f, 0.f}; }
            f2_t* part = (f2_t*)(smem + Q_PART);
            bf16x8 xr[3][2];
            TRLD(xr[0], 0); TRLD(xr[1], 1); TRLD(xr[2], 2);
#pragma unroll
            for (int i = 0; i < 16; ++i) {
                Wq[i] = wq[((i + 4 * hw) & 15) * 64];
                SB();
#pragma unroll
                for (int ti = 0; ti < 2; ++ti) {
                    ak[ti] = MFMA16(Wk[i], xr[i % 3][ti], ak[ti]);
                    av[ti] = MFMA16(xr[i % 3][ti], Wv[i], av[ti]);
                    if (i < 4) {
                        d1[ti] = MFMA16(ones, xr[i % 3][ti], d1[ti]);
                        d2[ti] = MFMA16(xr[i % 3][ti], xr[i % 3][ti], d2[ti]);
                    }
                    if (i == 6 + ti) {
                        const int rr = l15 & 3;
                        const float a2 = rr == 0 ? d2[ti][0] : rr == 1 ? d2[ti][1] : rr == 2 ? d2[ti][2] : d2[ti][3];
                        if (g == (l15 >> 2)) part[w * 32 + 16 * ti + l15] = f2_t{d1[ti][0], a2};
                    }
                }
                SB();
                if (i + 3 < 16) { TRLD(xr[i % 3], i + 3); }
            }
            BAR();
            STAMP(16 * layer + 8);
            f2_t st;
            {
                float x1 = 0.f, x2 = 0.f;
#pragma unroll
                for (int i = 0; i < 4; ++i) { f2_t v = part[(4 * kh + i) * 32 + (lane & 31)]; x1 += v[0]; x2 += v[1]; }
                float mean = x1 * (1.f / 512.f);
                float var = x2 * (1.f / 512.f) - mean * mean;
                st = ln_scalars(mean, fmaxf(var, 0.f));
            }
            float m0[2], r0[2];
            {
                u32x4 ko, vo;
#pragma unroll
                for (int i = 0; i < 2; ++i) {
                    m0[i] = __shfl(st[0], 16 * i + l15); r0[i] = __shfl(st[1], 16 * i + l15);
                    float kv[4], vv[4];
#pragma unroll
                    for (int r = 0; r < 4; ++r) {
                        kv[r] = (ak[i][r] - m0[i] * csk[r]) * r0[i];
                        const float m1 = __shfl(st[0], 16 * i + 4 * g + r), r1 = __shfl(st[1], 16 * i + 4 * g + r);
                        vv[r] = (av[i][r] - m1 * csv) * r1;
                    }
                    ko[2 * i] = pk(kv[0], kv[1]); ko[2 * i + 1] = pk(kv[2], kv[3]);
                    vo[2 * i] = pk(vv[0], vv[1]); vo[2 * i + 1] = pk(vv[2], vv[3]);
                }
                auto kx = __builtin_amdgcn_permlane16_swap(ko[0], ko[2], false, false);
                auto ky = __builtin_amdgcn_permlane16_swap(ko[1], ko[3], false, false);
                ko = sanit(u32x4{kx[0], ky[0], kx[1], ky[1]});
                auto vx = __builtin_amdgcn_permlane32_swap(vo[0], vo[2], false, false);
                auto vy = __builtin_amdgcn_permlane32_swap(vo[1], vo[3], false, false);
                vo = sanit(u32x4{vx[0], vy[0], vx[1], vy[1]});
                const unsigned go = kvb + (unsigned)(h * 64 + 2 * tt + kh) * 2048 + lane * 16;
                ST128(ko, rs, go);
                ST128(vo, rs, go + 1024);
                char* lo = smem + A_KV + hw * 20480 + (8 + kh) * 2048 + lane * 16;
                *(u32x4*)(lo) = ko;
                *(u32x4*)(lo + 1024) = vo;
                if (w == 0) *(u32x4*)(smem + A_ONES + lane * 16) = u32x4{0x3f803f80u, 0x3f803f80u, 0x3f803f80u, 0x3f803f80u};
            }
            STAMP(16 * layer + 6);
            f32x4 aq[2][2];
#pragma unroll
            for (int i = 0; i < 2; ++i) { aq[i][0] = f32x4{0.f, 0.f, 0.f, 0.f}; aq[i][1] = f32x4{0.f, 0.f, 0.f, 0.f}; }
            bf16x8 xq[6][2];
#pragma unroll
            for (int i = 0; i < 6; ++i) { TRLD(xq[i], i); }
#define KSRC(i) (ksrc0 + (i) * 1024)
#define KDST(i) (kdst0 + (i) * 1024)
            const char* ksrc0 = ws + kvb + (size_t)(h * 64 + P0) * 2048 + lane * 16;
            char* kdst0 = smem + A_KV + hw * 20480 + 4 * kh * 2048;
#pragma unroll
            for (int iq = 0; iq < 16; ++iq) {
                SB();
                if (iq == QD) DMA_ISSUE(8, KSRC, KDST, live);
                const int i = iq;
                SB();
#pragma unroll
                for (int ti = 0; ti < 2; ++ti) aq[ti][i & 1] = MFMA16(Wq[i], xq[i % 6][ti], aq[ti][i & 1]);
                SB();
                if (i + 6 < 16) { TRLD(xq[i % 6], i + 6); }
            }
            {
                const float qs = 0.25f * 1.4426950408889634f;
                u32x4 qo;
#pragma unroll
                for (int i = 0; i < 2; ++i) {
                    f32x4 v = aq[i][0] + aq[i][1];
                    float qv[4];
#pragma unroll
                    for (int r = 0; r < 4; ++r) qv[r] = rbf((v[r] - m0[i] * csq[r]) * r0[i]) * qs;
                    qo[2 * i] = pk(qv[0], qv[1]); qo[2 * i + 1] = pk(qv[2], qv[3]);
                }
                auto qx = __builtin_amdgcn_permlane16_swap(qo[0], qo[2], false, false);
                auto qy = __builtin_amdgcn_permlane16_swap(qo[1], qo[3], false, false);
                Qf = __builtin_bit_cast(bf16x8, (u32x4{qx[0], qy[0], qx[1], qy[1]}));
            }
            STAMP(16 * layer + 10);
        }
        bf16x8 Wo[16];
        {
            PHASE_IDS();
            {
                const char* ksrc0 = ws + kvb + (size_t)(h * 64 + P0) * 2048 + lane * 16;
                char* kdst0 = smem + A_KV + hw * 20480 + 4 * kh * 2048;
                if (QD < 16) { DMA_VALID2(8, KSRC, KDST, live); } else { DMA_VALID(8, KSRC, KDST, live); }
            }
            BAR();
            STAMP(16 * layer + 12);
            if (layer == 0) {
                const u32x4 pz = {POISON, POISON, POISON, POISON};
                ST128(pz, rs, OFF_XT3 + (unsigned)(b * 512 + tid) * 16);
                ST128(pz, rs, OFF_KV + 4 * MB + (unsigned)(b * 1024 + tid) * 16);
                ST128(pz, rs, OFF_KV + 4 * MB + (unsigned)(b * 1024 + 512 + tid) * 16);
                ST128(pz, rs, OFF_OF + 2 * MB + (unsigned)(b * 512 + tid) * 16);
            }
            const bf16x8* wo = (const bf16x8*)(ws + OFF_WOF) + (size_t)((4 * hg + hw) * 16) * 64 + lane;
            const int ab = 2 * tt + kh;
            u32x4 o = ab < 8 ? attn_body<true>(smem, hw, kh, lane, ab, Qf, wo, Wo) : attn_fast(smem, hw, kh, lane, Qf, wo, Wo);
            {
                const int tile = 2 * ab + (g & 1);
                const int gp = 2 * (h & 1) + (g >> 1);
                ST128(sanit(o), rs, ofb + (unsigned)((tile * 16 + (h >> 1)) * 64 + l15 + 16 * gp) * 16);
                SB();
#pragma unroll
                for (int ks = 8; ks < 16; ++ks) Wo[ks] = wo[ks * 64];
            }
            STAMP(16 * layer + 9);
            if (!OEARLY) BAR();
            STAMP(16 * layer + 13);
        }
        {
            PHASE_IDS();
            STAMP(16 * layer + 14);
            {
                const char* src0 = ws + ofb + (size_t)(4 * tt * 16 + 32 * kh + hw) * 1024 + lane * 16;
#define OSRC(i) (src0 + (i) * 4096)
#define ODST(i) (smem + 1024 * (32 * kh + hw + 4 * (i)))
                DMA_VALID(8, OSRC, ODST, 0xFFu);
            }
            BAR();
            STAMP(16 * layer + 11);
            if (layer == 0) { RLOAD(); SB(); }
            f32x4 acc[2][2];
#pragma unroll
            for (int i = 0; i < 2; ++i) { acc[i][0] = f32x4{0.f, 0.f, 0.f, 0.f}; acc[i][1] = f32x4{0.f, 0.f, 0.f, 0.f}; }
            {
                const char* op = smem + (2 * kh) * 16384 + lane * 16;
                bf16x8 of[2][8], og[2][8];
#pragma unroll
                for (int ks = 0; ks < 8; ++ks)
#pragma unroll
                    for (int i = 0; i < 2; ++i) of[i][ks] = *(const bf16x8*)(op + (i * 16 + ks) * 1024);
                SB();
#pragma unroll
                for (int ks = 0; ks < 8; ++ks) {
#pragma unroll
                    for (int i = 0; i < 2; ++i) og[i][ks] = *(const bf16x8*)(op + (i * 16 + 8 + ks) * 1024);
#pragma unroll
                    for (int i = 0; i < 2; ++i) acc[i][ks & 1] = MFMA16(of[i][ks], Wo[ks], acc[i][ks & 1]);
                    SB();
                }
#pragma unroll
                for (int ks = 0; ks < 8; ++ks)
#pragma unroll
                    for (int i = 0; i < 2; ++i) acc[i][ks & 1] = MFMA16(og[i][ks], Wo[8 + ks], acc[i][ks & 1]);
            }
            u32x2 o[2];
#pragma unroll
            for (int i = 0; i < 2; ++i) {
                f32x4 v = acc[i][0] + acc[i][1];
                o[i][0] = pk(bflo(xres[i][0]) + rbf(v[0]), bfhi(xres[i][0]) + rbf(v[1]));
                o[i][1] = pk(bflo(xres[i][1]) + rbf(v[2]), bfhi(xres[i][1]) + rbf(v[3]));
            }
            if (layer == 0) {
                float sel = 0.f;
#pragma unroll
                for (int i = 0; i < 2; ++i)
#pragma unroll
                    for (int r = 0; r < 4; ++r) {
                        const float x = (r & 1) ? bfhi(o[i][r >> 1]) : bflo(o[i][r >> 1]);
                        const float a1 = row16_sum(x), a2 = row16_sum(x * x);
                        if (l15 == 2 * (4 * i + r)) sel = a1;
                        if (l15 == 2 * (4 * i + r) + 1) sel = a2;
                    }
                {
                    const int e = l15 >> 1;
                    const int t = 64 * tt + 32 * kh + 16 * (e >> 2) + 4 * g + (e & 3);
                    const float sc = (l15 & 1) ? 16777216.0f : 1073741824.0f;
                    long long iv = (long long)__builtin_rintf(fminf(fmaxf(sel * sc, -1.0e15f), 1.0e15f)) + (1ll << 51) + (1ll << 58);
                    atomicAdd((unsigned long long*)(ws + OFF_ACC) + (size_t)t * 2 + (l15 & 1), (unsigned long long)iv);
                }
                SB();
                {
                    auto sx = __builtin_amdgcn_permlane16_swap(o[0][0], o[1][0], false, false);
                    auto sy = __builtin_amdgcn_permlane16_swap(o[0][1], o[1][1], false, false);
                    u32x4 ov = {sx[0], sy[0], sx[1], sy[1]};
                    ST128(sanit(ov), rs, OFF_XT2 + (unsigned)(64 * hg + 16 * hw + l15) * (T * 2) + 128 * tt + 64 * kh + 32 * (g & 1) + 16 * (g >> 1));
                }
                BAR();
            } else {
                BAR();
                u16* img = (u16*)smem;
#pragma unroll
                for (int i = 0; i < 2; ++i)
#pragma unroll
                    for (int r = 0; r < 4; ++r)
                        img[(32 * kh + 16 * i + 4 * g + r) * 72 + 16 * hw + l15] = (u16)((r & 1) ? (o[i][r >> 1] >> 16) : (o[i][r >> 1] & 0xffff));
                BAR();
                const int t = tid >> 3, nc = tid & 7;
                u32x4 r = *(const u32x4*)(img + t * 72 + 8 * nc);
                const __amdgpu_buffer_rsrc_t ro = __builtin_amdgcn_make_buffer_rsrc((void*)a.out, 0, T * D * 2, 0x00020000);
                ST128(r, ro, (unsigned)((64 * tt + t) * D + 64 * hg + 8 * nc) * 2u);
            }
            STAMP(16 * layer + 15);
        }
    }
}

__global__ __launch_bounds__(512, 2) void k_mega(MegaArgs a) {
    extern __shared__ __attribute__((aligned(16))) char smem[];
    char* ws = a.ws;
    STAMP(0);
    u32x4 Rv[4];
    layer_body<0>(a, smem, Rv);
    layer_body<1>(a, smem, Rv);
}

extern "C" void kernel_launch(void* const* d_in, const int* in_sizes, int n_in, void* d_out, int out_size, void* d_ws,
                              size_t ws_size, hipStream_t stream) {
    char* ws = (char*)d_ws;
    PrepArgs pa;
    pa.x = (const u16*)d_in[0];
    pa.wqkv = (const u16*)d_in[10];
    pa.wo = (const u16*)d_in[11];
    pa.Are = (const u16*)d_in[1];
    pa.Aim = (const u16*)d_in[2];
    pa.Bre = (const u16*)d_in[3];
    pa.Bim = (const u16*)d_in[4];
    pa.Cre = (const u16*)d_in[5];
    pa.Cim = (const u16*)d_in[6];
    pa.logdt = (const u16*)d_in[7];
    pa.ws = ws;
    MegaArgs ma;
    ma.Dskip = (const u16*)d_in[8];
    ma.out = (u16*)d_out;
    ma.ws = ws;
    (void)hipFuncSetAttribute((const void*)k_mega, hipFuncAttributeMaxDynamicSharedMemorySize, MEGA_LDS);
    k_prep<<<256, 512, 0, stream>>>(pa);
    k_mega<<<256, 512, MEGA_LDS, stream>>>(ma);
}
```

```cpp
#include <hip/hip_runtime.h>
#include <hip/hip_bf16.h>
#include <stdint.h>

typedef short bf16x8 __attribute__((ext_vector_type(8)));
typedef short bf16x4 __attribute__((ext_vector_type(4)));
typedef float f32x4 __attribute__((ext_vector_type(4)));
typedef float f32x16 __attribute__((ext_vector_type(16)));
typedef unsigned u32x4 __attribute__((ext_vector_type(4)));
typedef unsigned u32x2 __attribute__((ext_vector_type(2)));
typedef __bf16 bf2_t __attribute__((ext_vector_type(2)));
typedef float f2_t __attribute__((ext_vector_type(2)));
typedef unsigned short u16;
typedef __attribute__((address_space(3))) char lds_char;
typedef __attribute__((address_space(3))) unsigned lds_u32;

#define DEV __device__ __forceinline__
#define STAMP(i) do {} while (0)

#define PST(i) do {} while (0)
constexpr int T = 2048, D = 512, NM = 128;

constexpr unsigned MB = 1024 * 1024;
constexpr unsigned OFF_XT0 = 0, OFF_XT1 = 2 * MB, OFF_XT2 = 4 * MB, OFF_XT3 = 6 * MB;
constexpr unsigned OFF_WF = 8 * MB;
constexpr unsigned OFF_WOF = 9 * MB + 512 * 1024;
constexpr unsigned OFF_CS = 10 * MB;
constexpr unsigned OFF_ST0 = 10 * MB + 64 * 1024;
constexpr unsigned OFF_ACC = 10 * MB + 128 * 1024;
constexpr unsigned OFF_FLAGS = 10 * MB + 192 * 1024;
constexpr unsigned OFF_KV = 12 * MB;
constexpr unsigned OFF_OF = 20 * MB;
constexpr unsigned OFF_R = 24 * MB;
constexpr unsigned WS_USED = 28 * MB;

DEV float bf2f(unsigned v) { return __uint_as_float(v << 16); }
DEV float bflo(unsigned v) { return __uint_as_float(v << 16); }
DEV float bfhi(unsigned v) { return __uint_as_float(v & 0xffff0000u); }
DEV unsigned pk(float lo, float hi) {
    f2_t v = {lo, hi};
    return __builtin_bit_cast(unsigned, __builtin_convertvector(v, bf2_t));
}
DEV float rbf(float x) { return __uint_as_float(pk(x, 0.f) << 16); }
DEV float dot2(unsigned a, unsigned b, float acc) {
    return __builtin_amdgcn_fdot2_f32_bf16(__builtin_bit_cast(bf2_t, a), __builtin_bit_cast(bf2_t, b), acc, false);
}
DEV float dpp_ror8(float x) { return __builtin_bit_cast(float, __builtin_amdgcn_mov_dpp(__builtin_bit_cast(int, x), 0x128, 0xf, 0xf, true)); }
DEV float xrow16_max(float x) {
    auto s = __builtin_amdgcn_permlane16_swap(__float_as_uint(x), __float_as_uint(x), false, false);
    x = fmaxf(__uint_as_float(s[0]), __uint_as_float(s[1]));
    auto t = __builtin_amdgcn_permlane32_swap(__float_as_uint(x), __float_as_uint(x), false, false);
    return fmaxf(__uint_as_float(t[0]), __uint_as_float(t[1]));
}
DEV float xrow16_sum(float x) {
    auto s = __builtin_amdgcn_permlane16_swap(__float_as_uint(x), __float_as_uint(x), false, false);
    x = __uint_as_float(s[0]) + __uint_as_float(s[1]);
    auto t = __builtin_amdgcn_permlane32_swap(__float_as_uint(x), __float_as_uint(x), false, false);
    return __uint_as_float(t[0]) + __uint_as_float(t[1]);
}

#define MFMA16(a, b, c) __builtin_amdgcn_mfma_f32_16x16x32_bf16(a, b, c, 0, 0, 0)
#define MFMA32(a, b, c) __builtin_amdgcn_mfma_f32_32x32x16_bf16(a, b, c, 0, 0, 0)
#define MFMA16K16(a, b, c) __builtin_amdgcn_mfma_f32_16x16x16bf16_1k(a, b, c, 0, 0, 0)
#define SC1 16
#define LD128(rs, off) __builtin_amdgcn_raw_buffer_load_b128(rs, off, 0, SC1)
#define ST128(v, rs, off) __builtin_amdgcn_raw_buffer_store_b128(v, rs, off, 0, SC1)
#define VM0() asm volatile("s_waitcnt vmcnt(0)" ::: "memory")
typedef short v4s __attribute__((ext_vector_type(4)));
#define TRREAD(p) __builtin_amdgcn_ds_read_tr16_b64_v4i16((__attribute__((address_space(3))) v4s*)(p))
#define SB() __builtin_amdgcn_sched_barrier(0)
#define BAR() asm volatile("s_waitcnt lgkmcnt(0)\n\ts_barrier" ::: "memory")
#define PHASE_IDS() int tid = tid0; asm volatile("" : "+v"(tid)); const int lane = tid & 63, g = lane >> 4, l15 = lane & 15; (void)g; (void)l15;

DEV f2_t cexp_l(f2_t ef, float l) {
    f2_t t = ef * l;
    float e = __builtin_amdgcn_exp2f(t[0]);
    float fr = __builtin_amdgcn_fractf(t[1]);
    f2_t cs = {__builtin_amdgcn_cosf(fr), __builtin_amdgcn_sinf(fr)};
    return cs * e;
}
DEV void split2(f2_t v, unsigned& hi, unsigned& lo) {
    hi = pk(v[0], v[1]);
    f2_t hf = {bflo(hi), bfhi(hi)};
    f2_t r = v - hf;
    lo = pk(r[0], r[1]);
}
DEV float hival(int row) {
    int v;
    if (row < 16) v = (row & 7) + 128 * (row >> 3);
    else if (row < 24) { int i = row - 16; v = 256 + 2 * (i & 3) + 128 * (i >> 2); }
    else if (row < 32) { int i = row - 24; v = 512 + 4 * (i & 1) + 128 * (i >> 1); }
    else if (row < 40) v = 1024 + 128 * (row - 32);
    else if (row == 40) v = 2048;
    else v = 0;
    return (float)v;
}
DEV f2_t ln_scalars(float mean, float var) {
    float mb = rbf(mean);
    float vb = rbf(var);
    float sb = rbf(__builtin_amdgcn_sqrtf(rbf(vb + 1e-5f)));
    f2_t r = {mb, __builtin_amdgcn_rcpf(sb)};
    return r;
}

template <int CTRL> DEV float dppf(float x) { return __builtin_bit_cast(float, __builtin_amdgcn_mov_dpp(__builtin_bit_cast(int, x), CTRL, 0xf, 0xf, true)); }
DEV float row16_sum(float x) {
    x += dppf<0xB1>(x);
    x += dppf<0x4E>(x);
    x += dppf<0x124>(x);
    x += dppf<0x128>(x);
    return x;
}

constexpr unsigned POISON = 0xFFFFFFFFu;
struct PrepArgs {
    const u16 *x, *wqkv, *wo, *Are, *Aim, *Bre, *Bim, *Cre, *Cim, *logdt;
    char* ws;
};
__global__ __launch_bounds__(512) void k_prep(PrepArgs a) {
    char* __restrict__ ws = a.ws;
    __shared__ __attribute__((aligned(16))) char smem[8 * 648 * 4 + 2 * 648 * 4 + 64 * 144 + 128 * 80 + 1024];
    constexpr int P_X = 10 * 648 * 4, P_W = P_X + 64 * 144, P_CS = P_W + 128 * 80;
    const int tid = threadIdx.x, lane = tid & 63, w = tid >> 6;
    const int b = blockIdx.x;
    const int g = lane >> 4, l15 = lane & 15;
    PST(0);
    const int chl = w >> 2, kq4 = w & 3, ch = 2 * b + chl;
    u32x2 vAr[2], vAi[2], vBr[2], vBi[2], vCr[2], vCi[2];
#pragma unroll
    for (int ks = 0; ks < 2; ++ks) {
        const int n0 = ch * NM + 32 * kq4 + 16 * ks + 4 * g;
        vAr[ks] = *(const u32x2*)(a.Are + n0); vAi[ks] = *(const u32x2*)(a.Aim + n0);
        vBr[ks] = *(const u32x2*)(a.Bre + n0); vBi[ks] = *(const u32x2*)(a.Bim + n0);
        vCr[ks] = *(const u32x2*)(a.Cre + n0); vCi[ks] = *(const u32x2*)(a.Cim + n0);
    }
    const float ldt = bf2f(a.logdt[ch]);
    const u32x4 xrow = *(const u32x4*)(a.x + (size_t)(8 * b + w) * D + 8 * lane);
    const u32x4 xtile = *(const u32x4*)(a.x + (size_t)(64 * (b >> 3) + (tid >> 3)) * D + 64 * (b & 7) + 8 * (tid & 7));
    const bool isq = b < 192;
    const int id2 = isq ? b : b - 192;
    const int kq = id2 & 3, nb = id2 >> 2;
    const u32x4 wv = *(const u32x4*)((isq ? a.wqkv : a.wo) + (size_t)(128 * kq + (tid >> 2)) * (isq ? 1536 : 512) + 32 * nb + 8 * (tid & 3));
    const __amdgpu_buffer_rsrc_t rsp = __builtin_amdgcn_make_buffer_rsrc((void*)ws, 0, WS_USED, 0x00020000);
#define PSTORE(v, o) ST128(v, rsp, o)
#define NPM 4
#define KSPOS 6
#define PZ 0
    const u32x4 pz = {POISON, POISON, POISON, POISON};
#define POIS(k) ST128(pz, rsp, ((k) == 0 ? OFF_XT1 + (unsigned)(b * 512 + tid) * 16u : (k) == 1 ? OFF_XT2 + (unsigned)(b * 512 + tid) * 16u : \
    (k) == 2 ? OFF_KV + (unsigned)(b * 1024 + tid) * 16u : (k) == 3 ? OFF_KV + (unsigned)(b * 1024 + 512 + tid) * 16u : OFF_OF + (unsigned)(b * 512 + tid) * 16u))
    {
        u32x4 z = {0u, 0u, 0u, 0u};
        if (tid < 8) PSTORE(z, OFF_ACC + (unsigned)(b * 8 + tid) * 16u);
        POIS(0);
    }
    {
        const float dt = rbf(expf(ldt));
        PST(1);
        const float lo = (float)(8 * l15);
        float hv[3];
#pragma unroll
        for (int mt = 0; mt < 3; ++mt) hv[mt] = hival(16 * mt + l15);
        f32x4 acc[3];
#pragma unroll
        for (int mt = 0; mt < 3; ++mt) acc[mt] = f32x4{0.f, 0.f, 0.f, 0.f};
#pragma unroll
        for (int ks = 0; ks < 2; ++ks) {
            u32x4 Bh, Bl, Ah[3];
#pragma unroll
            for (int i = 0; i < 4; ++i) {
                if (4 * ks + i == KSPOS) {
                    SB();
    {
        *(u32x4*)(smem + P_X + (tid >> 3) * 144 + 16 * (tid & 7)) = xtile;
        *(u32x4*)(smem + P_W + (tid >> 2) * 80 + 16 * (tid & 3)) = wv;
        float s = 0.f;
#pragma unroll
        for (int j = 0; j < 4; ++j) s = dot2(xrow[j], 0x3f803f80u, s);
        s = xrow16_sum(row16_sum(s));
        const float mean = s * (1.f / 512.f);
        float q = 0.f;
#pragma unroll
        for (int j = 0; j < 4; ++j) { float d0 = bflo(xrow[j]) - mean, d1 = bfhi(xrow[j]) - mean; q += d0 * d0 + d1 * d1; }
        q = xrow16_sum(row16_sum(q));
        if (lane == 0) __builtin_amdgcn_raw_buffer_store_b64(__builtin_bit_cast(u32x2, ln_scalars(mean, q * (1.f / 512.f))), rsp, OFF_ST0 + (unsigned)(8 * b + w) * 8u, 0, SC1);
    }
    __syncthreads();
    {
        const int q = l15 >> 2, p = lane & 3;
        {
            const int d0 = 16 * (w & 3), t0 = 32 * (w >> 2) + 8 * g;
            const lds_char* src = (const lds_char*)smem + P_X + (t0 + q) * 144 + (d0 + 4 * p) * 2;
            u32x2 r0 = __builtin_bit_cast(u32x2, TRREAD(src)), r1 = __builtin_bit_cast(u32x2, TRREAD(src + 4 * 144));
            PSTORE((u32x4{r0[0], r0[1], r1[0], r1[1]}), OFF_XT0 + (unsigned)(64 * (b & 7) + d0 + l15) * (T * 2) + 2 * (64 * (b >> 3) + t0));
        }
        {
            const int nt2 = w & 1, k4 = w >> 1;
            const lds_char* src = (const lds_char*)smem + P_W + (32 * k4 + 8 * g + q) * 80 + (16 * nt2 + 4 * p) * 2;
            u32x2 r0 = __builtin_bit_cast(u32x2, TRREAD(src)), r1 = __builtin_bit_cast(u32x2, TRREAD(src + 4 * 80));
            u32x4 ov = {r0[0], r0[1], r1[0], r1[1]};
            const int n16 = 2 * nb + nt2, ks = 4 * kq + k4;
            size_t off;
            if (isq) off = OFF_WF + ((size_t)(((n16 & 31) * 3 + (n16 >> 5)) * 16 + ks) * 64 + lane) * 16;
            else off = OFF_WOF + ((size_t)(n16 * 16 + ks) * 64 + lane) * 16;
            PSTORE(ov, (unsigned)off);
            float cs = 0.f;
#pragma unroll
            for (int j = 0; j < 4; ++j) cs = dot2(ov[j], 0x3f803f80u, cs);
            cs = xrow16_sum(cs);
            if (g == 0) ((float*)(smem + P_CS))[w * 16 + l15] = cs;
        }
    }
                    SB();
                }
                const int wd = i >> 1;
                const bool hi = i & 1;
                float Ar = hi ? bfhi(vAr[ks][wd]) : bflo(vAr[ks][wd]), Ai = hi ? bfhi(vAi[ks][wd]) : bflo(vAi[ks][wd]);
                float Br = hi ? bfhi(vBr[ks][wd]) : bflo(vBr[ks][wd]), Bi = hi ? bfhi(vBi[ks][wd]) : bflo(vBi[ks][wd]);
                float Cr = hi ? bfhi(vCr[ks][wd]) : bflo(vCr[ks][wd]), Ci = hi ? bfhi(vCi[ks][wd]) : bflo(vCi[ks][wd]);
                const f2_t ef = {(dt * Ar) * 1.4426950408889634f, (dt * Ai) * 0.15915494309189535f};
                float dBr = dt * Br, dBi = dt * Bi;
                float cbr = Cr * dBr - Ci * dBi, cbi = Cr * dBi + Ci * dBr;
                f2_t e0 = cexp_l(ef, lo);
                f2_t gg = {cbr * e0[0] - cbi * e0[1], -(cbr * e0[1] + cbi * e0[0])};
                unsigned hh, ll;
                split2(gg, hh, ll);
                Bh[i] = hh; Bl[i] = ll;
#pragma unroll
                for (int mt = 0; mt < 3; ++mt) {
                    const f2_t ea = cexp_l(ef, hv[mt]);
                    Ah[mt][i] = pk(ea[0], ea[1]);
                }
            }
            bf16x8 bh = __builtin_bit_cast(bf16x8, Bh), bl = __builtin_bit_cast(bf16x8, Bl);
#pragma unroll
            for (int mt = 0; mt < 3; ++mt) {
                bf16x8 ah = __builtin_bit_cast(bf16x8, Ah[mt]);
                acc[mt] = MFMA16(ah, bl, acc[mt]);
                acc[mt] = MFMA16(ah, bh, acc[mt]);
            }
        }
        PST(2);
        float* kw = (float*)smem + w * 648;
        const int p0 = 4 * (g & 1) + 128 * (g >> 1) + 8 * l15;
        *(f32x4*)(kw + p0) = acc[0];
        if (g < 2) *(f32x4*)(kw + 256 + 64 * g + 4 * l15) = acc[1];
        else {
            const int pb = 384 + 64 * (g - 2) + 2 * l15;
            *(f2_t*)(kw + pb) = f2_t{acc[1][0], acc[1][1]};
            *(f2_t*)(kw + pb + 32) = f2_t{acc[1][2], acc[1][3]};
        }
        if (g < 2) {
#pragma unroll
            for (int r = 0; r < 4; ++r) kw[512 + 16 * (4 * g + r) + l15] = acc[2][r];
        } else if (lane == 32) kw[640] = acc[2][0];
    }
    __syncthreads();
    PST(3);
    {
        const float* kq_ = (const float*)smem;
        float* kt = (float*)smem + 8 * 648;
        if (tid < 2 * 162) {
            const int c = tid >= 162, pi = tid - 162 * c;
            const f32x4* kp = (const f32x4*)(kq_ + c * (4 * 648)) + pi;
            ((f32x4*)kt)[tid] = (kp[0] + kp[162]) + (kp[324] + kp[486]);
        }
        if (isq && tid >= 480) {
            const float* cp = (const float*)(smem + P_CS) + (tid - 480);
            __builtin_amdgcn_raw_buffer_store_b32(__float_as_uint((cp[0] + cp[32]) + (cp[64] + cp[96])), rsp, OFF_CS + (unsigned)(kq * 1536 + 32 * nb + (tid - 480)) * 4u, 0, SC1);
        }
        __syncthreads();
        if (tid < 8) PSTORE((u32x4{0u, 0u, 0u, 0u}), OFF_R + (unsigned)(2 * b + (tid >> 2)) * 4160 + 16 * (256 + (tid & 3)));
        {
            const int c = tid >> 8, ci = tid & 255;
            const int m = 255 - ci;
            int base; unsigned offs;
            if (m < 32) { base = 8 * m; offs = 0x76543210u; }
            else if (m < 64) { base = 256 + 4 * (m - 32); offs = 0x43222100u; }
            else if (m < 128) { base = 384 + 2 * (m - 64); offs = 0x22111000u; }
            else { base = 512 + (m - 128); offs = 0x11100000u | ((unsigned)(m & 1) << 16); }
            float v[8];
#pragma unroll
            for (int j = 0; j < 8; ++j) {
                const int i = 7 - j;
                v[j] = kt[c * 648 + base + ((offs >> (4 * i)) & 15)];
            }
            u32x4 o = {pk(v[0], v[1]), pk(v[2], v[3]), pk(v[4], v[5]), pk(v[6], v[7])};
            PSTORE(o, OFF_R + (unsigned)(2 * b + c) * 4160 + 16 * ci);
        }
    }
    PST(4);
}

constexpr int CV_RC = 4160, CV_RCH = 8 * CV_RC;
constexpr int CV_U0 = 2 * CV_RCH, CV_UPL = 1536, CV_UCH = 4 * CV_UPL;
constexpr int CV_Y0 = CV_U0 + 2 * CV_UCH;
constexpr int CV_YP = 64 * 144;
constexpr int Q_PART = 65536;
constexpr int A_KV = Q_PART + 2048;
constexpr int A_ONES = A_KV + 4 * 20480;
constexpr int MEGA_LDS = CV_Y0 + 8 * CV_YP;
static_assert(MEGA_LDS <= 163840 && A_ONES + 1024 <= MEGA_LDS, "lds");

struct MegaArgs {
    const u16* Dskip;
    u16* out;
    char* ws;
};

constexpr unsigned SPIN_MAX = 2000000u;
DEV u32x4 sanit(u32x4 v) {
#pragma unroll
    for (int j = 0; j < 4; ++j) v[j] = min(v[j], 0xFFFFFFFEu);
    return v;
}
DEV bool has_poison(u32x4 v) { return max(max(v[0], v[1]), max(v[2], v[3])) == POISON; }
#define DMA_VALID(N, SRC, DST, LIVE) { \
    unsigned todo_ = (LIVE); \
    VM0(); \
    for (unsigned spins_ = 0; todo_ != 0u && spins_ < SPIN_MAX; ++spins_) { \
        if (spins_) __builtin_amdgcn_s_sleep(4); \
        _Pragma("unroll") for (int i = 0; i < (N); ++i) \
            if (todo_ & (1u << i)) __builtin_amdgcn_global_load_lds((const unsigned*)(SRC(i)), (lds_u32*)(DST(i)), 16, 0, SC1); \
        VM0(); \
        u32x4 v_[N]; \
        _Pragma("unroll") for (int i = 0; i < (N); ++i) v_[i] = *(const u32x4*)((const char*)(DST(i)) + lane * 16); \
        unsigned bad_ = 0u; \
        _Pragma("unroll") for (int i = 0; i < (N); ++i) bad_ |= __any(has_poison(v_[i])) ? (1u << i) : 0u; \
        todo_ &= bad_; \
    } }
#define DMA_ISSUE(N, SRC, DST, LIVE) { \
    _Pragma("unroll") for (int i = 0; i < (N); ++i) \
        if ((LIVE) & (1u << i)) __builtin_amdgcn_global_load_lds((const unsigned*)(SRC(i)), (lds_u32*)(DST(i)), 16, 0, SC1); }
#define DMA_VALID2(N, SRC, DST, LIVE) { \
    unsigned todo_ = (LIVE); \
    for (unsigned spins_ = 0; todo_ != 0u && spins_ < SPIN_MAX; ++spins_) { \
        if (spins_ > 1) __builtin_amdgcn_s_sleep(4); \
        if (spins_) { \
            _Pragma("unroll") for (int i = 0; i < (N); ++i) \
                if (todo_ & (1u << i)) __builtin_amdgcn_global_load_lds((const unsigned*)(SRC(i)), (lds_u32*)(DST(i)), 16, 0, SC1); \
        } \
        VM0(); \
        u32x4 v_[N]; \
        _Pragma("unroll") for (int i = 0; i < (N); ++i) v_[i] = *(const u32x4*)((const char*)(DST(i)) + lane * 16); \
        unsigned bad_ = 0u; \
        _Pragma("unroll") for (int i = 0; i < (N); ++i) bad_ |= __any(has_poison(v_[i])) ? (1u << i) : 0u; \
        todo_ &= bad_; \
    } \
    VM0(); }
DEV float max3f(float a, float b, float c) { return __builtin_fmaxf(__builtin_fmaxf(a, b), c); }
DEV float max2f(float a, float b) { return __builtin_fmaxf(a, b); }
template <bool EDGE>
DEV u32x4 attn_body(const char* smem, int hw, int kh, int lane, int ab, const bf16x8 Qf, const bf16x8* wo, bf16x8 (&Wo)[16]) {
    const int r32 = lane & 31, hh = lane >> 5;
    const float NEG = -1.0e30f;
    const char* kb = smem + A_KV + hw * 20480 + kh * 2048 + lane * 16;
    const char* vb = (r32 < 16 ? smem + A_KV + hw * 20480 + kh * 2048 + 1024 : smem + A_ONES) + hh * 256 + (r32 & 15) * 16;
    const int vstep = r32 < 16 ? 2048 : 0;
    bf16x8 Kf[9];
#pragma unroll
    for (int j = 0; j < 9; ++j) Kf[j] = *(const bf16x8*)(kb + j * 2048);
    f32x16 zero;
#pragma unroll
    for (int i = 0; i < 16; ++i) zero[i] = 0.f;
    float m0 = NEG, m1 = NEG;
    {
        f32x16 Sn = MFMA32(Kf[0], Qf, zero);
#pragma unroll
        for (int j = 0; j < 9; ++j) {
            f32x16 S = Sn;
            if (j < 8) Sn = MFMA32(Kf[j + 1], Qf, zero);
            SB();
            if (j == 0) {
#pragma unroll
                for (int i = 0; i < 16; ++i) if (!((i & 3) + 8 * (i >> 2) + 4 * hh > r32)) S[i] = NEG;
            }
            if (j == 8) {
#pragma unroll
                for (int i = 0; i < 16; ++i) if ((i & 3) + 8 * (i >> 2) + 4 * hh > r32) S[i] = NEG;
            }
            float a0 = m0, a1 = m1;
#pragma unroll
            for (int i = 0; i < 16; i += 4) { a0 = max3f(a0, S[i], S[i + 1]); a1 = max3f(a1, S[i + 2], S[i + 3]); }
            if (EDGE) { if (ab - 8 + j >= 0) { m0 = a0; m1 = a1; } }
            else { m0 = a0; m1 = a1; }
            SB();
        }
    }
    float m = max2f(m0, m1);
    {
        auto t = __builtin_amdgcn_permlane32_swap(__float_as_uint(m), __float_as_uint(m), false, false);
        m = max2f(__uint_as_float(t[0]), __uint_as_float(t[1]));
    }
    f32x16 nm;
#pragma unroll
    for (int i = 0; i < 16; ++i) nm[i] = -m;
    f32x16 O = zero;
    {
        f32x16 Sn = MFMA32(Kf[0], Qf, nm);
        bf16x8 vn0 = *(const bf16x8*)(vb), vn1 = *(const bf16x8*)(vb + 512);
#pragma unroll
        for (int j = 0; j < 9; ++j) {
            f32x16 S = Sn;
            const bf16x8 v0 = vn0, v1 = vn1;
            if (j < 8) {
                Sn = MFMA32(Kf[j + 1], Qf, nm);
                vn0 = *(const bf16x8*)(vb + (j + 1) * vstep); vn1 = *(const bf16x8*)(vb + (j + 1) * vstep + 512);
                Wo[j] = wo[j * 64];
            }
            SB();
            if (j == 0) {
#pragma unroll
                for (int i = 0; i < 16; ++i) if (!((i & 3) + 8 * (i >> 2) + 4 * hh > r32)) S[i] = NEG;
            }
            if (j == 8) {
#pragma unroll
                for (int i = 0; i < 16; ++i) if ((i & 3) + 8 * (i >> 2) + 4 * hh > r32) S[i] = NEG;
            }
            u32x4 p0, p1;
#pragma unroll
            for (int i = 0; i < 4; ++i) p0[i] = pk(__builtin_amdgcn_exp2f(S[2 * i]), __builtin_amdgcn_exp2f(S[2 * i + 1]));
            if (!EDGE || ab - 8 + j >= 0) O = MFMA32(v0, __builtin_bit_cast(bf16x8, p0), O);
#pragma unroll
            for (int i = 0; i < 4; ++i) p1[i] = pk(__builtin_amdgcn_exp2f(S[8 + 2 * i]), __builtin_amdgcn_exp2f(S[8 + 2 * i + 1]));
            if (!EDGE || ab - 8 + j >= 0) O = MFMA32(v1, __builtin_bit_cast(bf16x8, p1), O);
            SB();
        }
    }
    const float inv = __builtin_amdgcn_rcpf(O[8]);
    const unsigned a0 = pk(O[0] * inv, O[1] * inv), a1 = pk(O[2] * inv, O[3] * inv);
    const unsigned b0 = pk(O[4] * inv, O[5] * inv), b1 = pk(O[6] * inv, O[7] * inv);
    auto sx = __builtin_amdgcn_permlane32_swap(a0, b0, false, false);
    auto sy = __builtin_amdgcn_permlane32_swap(a1, b1, false, false);
    return u32x4{sx[0], sy[0], sx[1], sy[1]};
}

DEV u32x4 attn_fast(const char* smem, int hw, int kh, int lane, const bf16x8 Qf, const bf16x8* wo, bf16x8 (&Wo)[16]) {
    const int r32 = lane & 31, hh = lane >> 5;
    const char* kb = smem + A_KV + hw * 20480 + kh * 2048 + lane * 16;
    const char* vb = (r32 < 16 ? smem + A_KV + hw * 20480 + kh * 2048 + 1024 : smem + A_ONES) + hh * 256 + (r32 & 15) * 16;
    const int vstep = r32 < 16 ? 2048 : 0;
    bf16x8 Kf[9];
#pragma unroll
    for (int j = 0; j < 9; ++j) Kf[j] = *(const bf16x8*)(kb + j * 2048);
    f32x16 zero;
#pragma unroll
    for (int i = 0; i < 16; ++i) zero[i] = 0.f;
    float m0, m1;
    {
        f32x16 Sa = MFMA32(Kf[0], Qf, zero), Sb = MFMA32(Kf[8], Qf, zero);
        f32x16 Sn = MFMA32(Kf[1], Qf, zero);
        SB();
#pragma unroll
        for (int i = 0; i < 16; ++i) Sa[i] = ((i & 3) + 8 * (i >> 2) + 4 * hh > r32) ? Sa[i] : Sb[i];
        m0 = max3f(Sa[0], Sa[1], Sa[2]); m1 = max3f(Sa[3], Sa[4], Sa[5]);
        m0 = max3f(m0, Sa[6], Sa[7]);
#pragma unroll
        for (int i = 8; i < 16; i += 4) { m0 = max3f(m0, Sa[i], Sa[i + 1]); m1 = max3f(m1, Sa[i + 2], Sa[i + 3]); }
        SB();
#pragma unroll
        for (int j = 1; j < 8; ++j) {
            f32x16 S = Sn;
            if (j < 7) Sn = MFMA32(Kf[j + 1], Qf, zero);
            SB();
#pragma unroll
            for (int i = 0; i < 16; i += 4) { m0 = max3f(m0, S[i], S[i + 1]); m1 = max3f(m1, S[i + 2], S[i + 3]); }
            SB();
        }
    }
    float m = max2f(m0, m1);
    {
        auto t = __builtin_amdgcn_permlane32_swap(__float_as_uint(m), __float_as_uint(m), false, false);
        m = max2f(__uint_as_float(t[0]), __uint_as_float(t[1]));
    }
    f32x16 nm;
#pragma unroll
    for (int i = 0; i < 16; ++i) nm[i] = -m;
    f32x16 O = zero;
    {
        f32x16 Sn = MFMA32(Kf[1], Qf, nm);
        bf16x8 vn0 = *(const bf16x8*)(vb + vstep), vn1 = *(const bf16x8*)(vb + vstep + 512);
        f32x16 Sb;
        bf16x8 vb0, vb1;
#pragma unroll
        for (int j = 1; j < 8; ++j) {
            f32x16 S = Sn;
            const bf16x8 v0 = vn0, v1 = vn1;
            const int jn = j < 7 ? j + 1 : 0;
            Sn = MFMA32(Kf[jn], Qf, nm);
            vn0 = *(const bf16x8*)(vb + jn * vstep); vn1 = *(const bf16x8*)(vb + jn * vstep + 512);
            if (j == 7) { Sb = MFMA32(Kf[8], Qf, nm); vb0 = *(const bf16x8*)(vb + 8 * vstep); vb1 = *(const bf16x8*)(vb + 8 * vstep + 512); }
            Wo[j - 1] = wo[(j - 1) * 64];
            if (j == 7) Wo[7] = wo[7 * 64];
            SB();
            u32x4 p0, p1;
#pragma unroll
            for (int i = 0; i < 4; ++i) p0[i] = pk(__builtin_amdgcn_exp2f(S[2 * i]), __builtin_amdgcn_exp2f(S[2 * i + 1]));
            O = MFMA32(v0, __builtin_bit_cast(bf16x8, p0), O);
#pragma unroll
            for (int i = 0; i < 4; ++i) p1[i] = pk(__builtin_amdgcn_exp2f(S[8 + 2 * i]), __builtin_amdgcn_exp2f(S[8 + 2 * i + 1]));
            O = MFMA32(v1, __builtin_bit_cast(bf16x8, p1), O);
            SB();
        }
        u32x4 pa[2], pb[2];
#pragma unroll
        for (int i = 0; i < 8; ++i) {
            const int k0 = ((2 * i) & 3) + 8 * ((2 * i) >> 2) + 4 * hh;
            const bool f0 = k0 > r32, f1 = k0 + 1 > r32;
            const unsigned pp = pk(__builtin_amdgcn_exp2f(f0 ? Sn[2 * i] : Sb[2 * i]), __builtin_amdgcn_exp2f(f1 ? Sn[2 * i + 1] : Sb[2 * i + 1]));
            const unsigned msk = (f0 ? 0xffffu : 0u) | (f1 ? 0xffff0000u : 0u);
            pa[i >> 2][i & 3] = pp & msk;
            pb[i >> 2][i & 3] = pp & ~msk;
        }
        O = MFMA32(vn0, __builtin_bit_cast(bf16x8, pa[0]), O);
        O = MFMA32(vn1, __builtin_bit_cast(bf16x8, pa[1]), O);
        O = MFMA32(vb0, __builtin_bit_cast(bf16x8, pb[0]), O);
        O = MFMA32(vb1, __builtin_bit_cast(bf16x8, pb[1]), O);
    }
    const float inv = __builtin_amdgcn_rcpf(O[8]);
    const unsigned a0 = pk(O[0] * inv, O[1] * inv), a1 = pk(O[2] * inv, O[3] * inv);
    const unsigned b0 = pk(O[4] * inv, O[5] * inv), b1 = pk(O[6] * inv, O[7] * inv);
    auto sx = __builtin_amdgcn_permlane32_swap(a0, b0, false, false);
    auto sy = __builtin_amdgcn_permlane32_swap(a1, b1, false, false);
    return u32x4{sx[0], sy[0], sx[1], sy[1]};
}

#define QD 16
#define OEARLY 0
#define NE (layer == 0 ? 0 : 8)
#define RLOAD() { const int ci2_ = 256 + (tid & 3), c2_ = (tid >> 2) & 1; \
    const u32x4* src_ = (const u32x4*)(ws + OFF_R + (size_t)(2 * b + (tid >> 8)) * 4160 + 16 * (tid & 255)); \
    const u32x4* src2_ = (const u32x4*)(ws + OFF_R + (size_t)(2 * b + c2_) * 4160 + 16 * (ci2_ > 258 ? 258 : ci2_)); \
    Rv[0] = src_[0]; Rv[1] = src_[1]; Rv[2] = src2_[0]; Rv[3] = src2_[1]; }
#define WLOAD(id_) { const int i_ = (id_) >> 1; \
    if ((id_) & 1) Wv[i_] = wf[(32 + ((i_ + 4 * hw) & 15)) * 64]; \
    else Wk[i_] = wf[(16 + ((i_ + 4 * hw) & 15)) * 64]; }
template <int layer>
DEV void layer_body(const MegaArgs& a, char* smem, u32x4 (&Rv)[4]) {
    char* ws = a.ws;
    const int tid0 = threadIdx.x;
    const int w = __builtin_amdgcn_readfirstlane(tid0 >> 6);
    const int b = blockIdx.x;
    const int tt = 4 * (b & 7) + ((b >> 3) & 3), hg = b >> 5;
    const int hw = w & 3, kh = w >> 2;
    const int h = 4 * hg + hw;
    const __amdgpu_buffer_rsrc_t rs = __builtin_amdgcn_make_buffer_rsrc((void*)ws, 0, WS_USED, 0x00020000);
    {
        const unsigned xin = layer == 0 ? OFF_XT0 : OFF_XT2;
        const unsigned xmid = layer == 0 ? OFF_XT1 : OFF_XT3;
        const unsigned kvb = OFF_KV + layer * 4 * MB;
        const unsigned ofb = OFF_OF + layer * 2 * MB;
        bf16x8 Wk[16], Wv[16];
        f32x4 csq = {0.f, 0.f, 0.f, 0.f}, csk = {0.f, 0.f, 0.f, 0.f};
        float csv = 0.f;
        {
            PHASE_IDS();
            const int c = tid >> 8, ti = tid & 255;
            u32x4 xv, sv[4], s0v[2], pf[8];
            if (layer == 0) {
                xv = *(const u32x4*)(ws + xin + (size_t)(2 * b + c) * (T * 2) + 16 * ti);
                s0v[0] = *(const u32x4*)(ws + OFF_ST0 + 32 * tid);
                s0v[1] = *(const u32x4*)(ws + OFF_ST0 + 32 * tid + 16);
            }
            const float dk = bf2f(a.Dskip[2 * b + c]);
            {
                const int ci2 = 256 + (tid & 3), c2 = (tid >> 2) & 1;
                if (layer == 0) {
                    RLOAD(); SB();
                    const u32x4 pz = {POISON, POISON, POISON, POISON};
                    if (NPM >= 1) ST128(pz, rs, OFF_XT2 + (unsigned)(b * 512 + tid) * 16u);
                    if (NPM >= 2) ST128(pz, rs, OFF_OF + (unsigned)(b * 512 + tid) * 16u);
                    if (NPM >= 3) ST128(pz, rs, OFF_KV + (unsigned)(b * 1024 + 512 + tid) * 16u);
                    if (NPM >= 4) ST128(pz, rs, OFF_KV + (unsigned)(b * 1024 + tid) * 16u);
                    SB();
                }
                const u32x4 v0 = Rv[0], v1 = Rv[1], z0 = Rv[2], z1 = Rv[3];
#pragma unroll
                for (int rep = 0; rep < 2; ++rep) {
                    if (rep == 1 && tid >= 8) break;
                    unsigned dw[8];
#pragma unroll
                    for (int j = 0; j < 4; ++j) { dw[j] = rep ? z0[j] : v0[j]; dw[4 + j] = rep ? z1[j] : v1[j]; }
                    char* dst = smem + (rep ? c2 * CV_RCH + 16 * (ci2 > 258 ? 258 : ci2) : c * CV_RCH + 16 * ti);
#pragma unroll
                    for (int s = 0; s < 8; ++s) {
                        u32x4 o;
#pragma unroll
                        for (int d = 0; d < 4; ++d) {
                            const int k = (s >> 1) + d;
                            o[d] = (s & 1) ? __builtin_amdgcn_alignbyte(dw[k + 1], dw[k], 2) : dw[k];
                        }
                        *(u32x4*)(dst + s * CV_RC) = o;
                    }
                }
                if (tid < 256) *(u32x4*)(smem + CV_U0 + (tid >> 5) * CV_UPL + 16 * (tid & 31)) = u32x4{0u, 0u, 0u, 0u};
            }
            STAMP(16 * layer + 1);
            const bf16x8* wf = (const bf16x8*)(ws + OFF_WF) + (size_t)(h * 3) * 16 * 64 + lane;
            f32x4* stl = (f32x4*)(smem + CV_Y0);
            if (layer != 0) {
                u32x4 av[4];
                VM0();
                bool bad = true;
                for (unsigned spins = 0; spins < SPIN_MAX; ++spins) {
                    asm volatile("" ::: "memory");
                    if (bad) {
                        xv = LD128(rs, xin + (unsigned)(2 * b + c) * (T * 2) + 16 * ti);
                        bad = has_poison(xv);
                    }
                    if (!__any(bad)) break;
                    __builtin_amdgcn_s_sleep(4);
                }
                BAR();
                bad = true;
                for (unsigned spins = 0; spins < SPIN_MAX; ++spins) {
                    asm volatile("" ::: "memory");
                    if (bad) {
#pragma unroll
                        for (int j = 0; j < 4; ++j) av[j] = LD128(rs, OFF_ACC + 8192 * j + 16 * tid);
                        bad = false;
#pragma unroll
                        for (int j = 0; j < 4; ++j) bad |= (av[j][1] >> 26) != 32u || (av[j][3] >> 26) != 32u;
                    }
                    if (!__any(bad)) break;
                    __builtin_amdgcn_s_sleep(8);
                }
                STAMP(16 + 2);
#pragma unroll
                for (int j = 0; j < 4; ++j) {
#pragma unroll
                    for (int id = j * NE / 4; id < (j + 1) * NE / 4; ++id) WLOAD(id);
                    SB();
                    const float h1 = (float)(int)((av[j][1] & 0x03FFFFFFu) - 0x01000000u), h2 = (float)(int)((av[j][3] & 0x03FFFFFFu) - 0x01000000u);
                    const float m1 = (float)(av[j][0] >> 16), m2 = (float)(av[j][2] >> 16);
                    const float l1 = (float)(av[j][0] & 0xFFFFu), l2 = (float)(av[j][2] & 0xFFFFu);
                    const float mean = __builtin_fmaf(__builtin_fmaf(h1, 65536.f, m1), 65536.f, l1) * (1.f / (1073741824.f * 512.f));
                    const float ex2 = __builtin_fmaf(__builtin_fmaf(h2, 65536.f, m2), 65536.f, l2) * (1.f / (16777216.f * 512.f));
                    ((f2_t*)stl)[512 * j + tid] = ln_scalars(mean, fmaxf(__builtin_fmaf(-mean, mean, ex2), 0.f));
                }
            } else {
                stl[2 * tid] = __builtin_bit_cast(f32x4, s0v[0]);
                stl[2 * tid + 1] = __builtin_bit_cast(f32x4, s0v[1]);
                SB();
#pragma unroll
                for (int id = 0; id < NE; ++id) WLOAD(id);
                SB();
            }
            BAR();
#pragma unroll
            for (int j = 0; j < 4; ++j) sv[j] = __builtin_bit_cast(u32x4, stl[4 * ti + j]);
            u32x4 uv;
#pragma unroll
            for (int j = 0; j < 4; ++j) {
                float u0 = rbf(bflo(xv[j]) - __uint_as_float(sv[j][0])) * __uint_as_float(sv[j][1]);
                float u1 = rbf(bfhi(xv[j]) - __uint_as_float(sv[j][2])) * __uint_as_float(sv[j][3]);
                uv[j] = pk(u0, u1);
            }
            *(u32x4*)(smem + CV_U0 + c * CV_UCH + (ti & 3) * CV_UPL + 16 * (32 + (ti >> 2))) = uv;
            BAR();
            STAMP(16 * layer + 3);
            const float* cs = (const float*)(ws + OFF_CS);
            f32x4 cq[4], ck[4];
            float cv[4];
            {
                const int cw = w >> 2, kk = w & 1, par = (w >> 1) & 1;
                const int r = lane & 31, hh = lane >> 5;
                const int rho = r >> 3, cc = r & 7;
                const char* pa = smem + cw * CV_RCH + (7 - cc) * CV_RC + 16 * (255 + 2 * kk - rho + hh) - 64 * par;
                const char* pb = smem + CV_U0 + cw * CV_UCH + (2 * kk + hh) * CV_UPL + 16 * (32 + r) - 16 * par;
                f32x16 acc0, acc1;
#pragma unroll
                for (int i = 0; i < 16; ++i) { acc0[i] = 0.f; acc1[i] = 0.f; }
                bf16x8 a0 = *(const bf16x8*)(pa), a1 = *(const bf16x8*)(pa - 2048);
                bf16x8 b0 = *(const bf16x8*)(pb), b1 = *(const bf16x8*)(pb + 512);
#pragma unroll
                for (int it = 0; it < 16; ++it) {
                    bf16x8 na0, na1, nb0, nb1;
                    if (it < 15) {
                        na0 = *(const bf16x8*)(pa - 128 * (it + 1));
                        na1 = *(const bf16x8*)(pa - 128 * (it + 1) - 2048);
                        nb0 = *(const bf16x8*)(pb - 32 * (it + 1));
                        nb1 = *(const bf16x8*)(pb - 32 * (it + 1) + 512);
                    }
#pragma unroll
                    for (int id = NE + it * (32 - NE) / 16; id < NE + (it + 1) * (32 - NE) / 16; ++id) WLOAD(id);
                    SB();
                    acc0 = MFMA32(a0, b0, acc0);
                    acc1 = MFMA32(a0, b1, acc1);
                    acc1 = MFMA32(a1, b0, acc1);
                    SB();
                    if (it < 15) { a0 = na0; a1 = na1; b0 = nb0; b1 = nb1; }
                }
                char* yp = smem + CV_Y0 + w * CV_YP + r * 144 + 16 * hh;
#pragma unroll
                for (int q = 0; q < 4; ++q) {
                    f32x4 v0 = {acc0[4 * q], acc0[4 * q + 1], acc0[4 * q + 2], acc0[4 * q + 3]};
                    f32x4 v1 = {acc1[4 * q], acc1[4 * q + 1], acc1[4 * q + 2], acc1[4 * q + 3]};
                    *(f32x4*)(yp + 32 * q) = v0;
                    *(f32x4*)(yp + 32 * 144 + 32 * q) = v1;
                }
                SB();
                if (layer == 0) VM0();
#pragma unroll
                for (int kq = 0; kq < 4; ++kq) {
                    cq[kq] = *(const f32x4*)(cs + kq * 1536 + h * 16 + 4 * g);
                    ck[kq] = *(const f32x4*)(cs + kq * 1536 + 512 + h * 16 + 4 * g);
                    cv[kq] = cs[kq * 1536 + 1024 + h * 16 + l15];
                }
                SB();
            }
            STAMP(16 * layer + 4);
            BAR();
            {
                const char* y0 = smem + CV_Y0 + (4 * c) * CV_YP + (ti >> 2) * 144 + (ti & 3) * 32;
                f32x4 pp[8];
#pragma unroll
                for (int p = 0; p < 4; ++p) { pp[2 * p] = *(const f32x4*)(y0 + p * CV_YP); pp[2 * p + 1] = *(const f32x4*)(y0 + p * CV_YP + 16); }
                f32x4 ya = (pp[0] + pp[2]) + (pp[4] + pp[6]), yb = (pp[1] + pp[3]) + (pp[5] + pp[7]);
                float y[8] = {ya[0], ya[1], ya[2], ya[3], yb[0], yb[1], yb[2], yb[3]};
                u32x4 o;
#pragma unroll
                for (int j = 0; j < 4; ++j) {
                    float s0 = rbf(rbf(y[2 * j]) + rbf(bflo(uv[j]) * dk));
                    float s1 = rbf(rbf(y[2 * j + 1]) + rbf(bfhi(uv[j]) * dk));
                    o[j] = pk(bflo(xv[j]) + s0, bfhi(xv[j]) + s1);
                }
                ST128(sanit(o), rs, xmid + (unsigned)(2 * b + c) * (T * 2) + 16 * ti);
                SB();
                csq = (cq[0] + cq[1]) + (cq[2] + cq[3]);
                csk = (ck[0] + ck[1]) + (ck[2] + ck[3]);
                csv = (cv[0] + cv[1]) + (cv[2] + cv[3]);
            }
            BAR();
            STAMP(16 * layer + 5);
        }
        u32x2 xres[2];
        bf16x8 Qf;
        const int P0 = 2 * tt - 8 + 4 * kh;
        const unsigned live = P0 >= 0 ? 0xFFu : (P0 <= -4 ? 0u : (0xFFu << (2 * (-P0))) & 0xFFu);
        {
            PHASE_IDS();
            {
                const int rr = lane >> 3, chp = lane & 7;
                const int f = ((rr >> 1) & 1) | ((w & 1) << 1);
                const char* src0 = ws + xmid + (size_t)(8 * w + rr) * (T * 2) + 128 * tt + 16 * (chp ^ (2 * f));
#define XSRC(i) (src0 + (size_t)(i) * (64 * T * 2))
#define XDST(i) (smem + 1024 * (w + 8 * (i)))
                DMA_VALID(8, XSRC, XDST, 0xFFu);
            }
            BAR();
            STAMP(16 * layer + 7);
            {
                const int d = 64 * hg + 16 * hw + l15;
                const int f = ((d >> 1) & 1) | (((d >> 3) & 1) << 1);
#pragma unroll
                for (int i = 0; i < 2; ++i)
                    xres[i] = *(const u32x2*)(smem + d * 128 + 16 * ((4 * kh + 2 * i + (g >> 1)) ^ (2 * f)) + 8 * (g & 1));
            }
            const int q = l15 >> 2, p = lane & 3;
            const int f = ((q >> 1) & 1) | ((g & 1) << 1);
            const lds_char* lbase = (const lds_char*)smem + (8 * g + q) * 128 + 8 * (p & 1);
            const bf16x8* wq = (const bf16x8*)(ws + OFF_WF) + (size_t)(h * 3) * 16 * 64 + lane;
            bf16x8 Wq[16];
#define TRLD(dst, i_) { const lds_char* lb_ = lbase + (((i_) + 4 * hw) & 15) * 4096; _Pragma("unroll") for (int ti = 0; ti < 2; ++ti) { \
                const int ch = (2 * (2 * kh + ti) + (p >> 1)) ^ (2 * f); \
                v4s x0 = TRREAD(lb_ + 16 * ch); \
                v4s x1 = TRREAD(lb_ + 512 + 16 * ch); \
                dst[ti] = bf16x8{x0[0], x0[1], x0[2], x0[3], x1[0], x1[1], x1[2], x1[3]}; } }
            f32x4 ak[2], av[2];
            f32x4 d1[2] = {f32x4{0.f, 0.f, 0.f, 0.f}, f32x4{0.f, 0.f, 0.f, 0.f}}, d2[2] = {f32x4{0.f, 0.f, 0.f, 0.f}, f32x4{0.f, 0.f, 0.f, 0.f}};
            const bf16x8 ones = {0x3f80, 0x3f80, 0x3f80, 0x3f80, 0x3f80, 0x3f80, 0x3f80, 0x3f80};
#pragma unroll
            for (int i = 0; i < 2; ++i) { ak[i] = f32x4{0.f, 0.f, 0.f, 0.f}; av[i] = f32x4{0.f, 0.f, 0.f, 0.f}; }
            f2_t* part = (f2_t*)(smem + Q_PART);
            bf16x8 xr[3][2];
            TRLD(xr[0], 0); TRLD(xr[1], 1); TRLD(xr[2], 2);
#pragma unroll
            for (int i = 0; i < 16; ++i) {
                Wq[i] = wq[((i + 4 * hw) & 15) * 64];
                SB();
#pragma unroll
                for (int ti = 0; ti < 2; ++ti) {
                    ak[ti] = MFMA16(Wk[i], xr[i % 3][ti], ak[ti]);
                    av[ti] = MFMA16(xr[i % 3][ti], Wv[i], av[ti]);
                    if (i < 4) {
                        d1[ti] = MFMA16(ones, xr[i % 3][ti], d1[ti]);
                        d2[ti] = MFMA16(xr[i % 3][ti], xr[i % 3][ti], d2[ti]);
                    }
                    if (i == 6 + ti) {
                        const int rr = l15 & 3;
                        const float a2 = rr == 0 ? d2[ti][0] : rr == 1 ? d2[ti][1] : rr == 2 ? d2[ti][2] : d2[ti][3];
                        if (g == (l15 >> 2)) part[w * 32 + 16 * ti + l15] = f2_t{d1[ti][0], a2};
                    }
                }
                SB();
                if (i + 3 < 16) { TRLD(xr[i % 3], i + 3); }
            }
            BAR();
            STAMP(16 * layer + 8);
            f2_t st;
            {
                float x1 = 0.f, x2 = 0.f;
#pragma unroll
                for (int i = 0; i < 4; ++i) { f2_t v = part[(4 * kh + i) * 32 + (lane & 31)]; x1 += v[0]; x2 += v[1]; }
                float mean = x1 * (1.f / 512.f);
                float var = x2 * (1.f / 512.f) - mean * mean;
                st = ln_scalars(mean, fmaxf(var, 0.f));
            }
            float m0[2], r0[2];
            {
                u32x4 ko, vo;
#pragma unroll
                for (int i = 0; i < 2; ++i) {
                    m0[i] = __shfl(st[0], 16 * i + l15); r0[i] = __shfl(st[1], 16 * i + l15);
                    float kv[4], vv[4];
#pragma unroll
                    for (int r = 0; r < 4; ++r) {
                        kv[r] = (ak[i][r] - m0[i] * csk[r]) * r0[i];
                        const float m1 = __shfl(st[0], 16 * i + 4 * g + r), r1 = __shfl(st[1], 16 * i + 4 * g + r);
                        vv[r] = (av[i][r] - m1 * csv) * r1;
                    }
                    ko[2 * i] = pk(kv[0], kv[1]); ko[2 * i + 1] = pk(kv[2], kv[3]);
                    vo[2 * i] = pk(vv[0], vv[1]); vo[2 * i + 1] = pk(vv[2], vv[3]);
                }
                auto kx = __builtin_amdgcn_permlane16_swap(ko[0], ko[2], false, false);
                auto ky = __builtin_amdgcn_permlane16_swap(ko[1], ko[3], false, false);
                ko = sanit(u32x4{kx[0], ky[0], kx[1], ky[1]});
                auto vx = __builtin_amdgcn_permlane32_swap(vo[0], vo[2], false, false);
                auto vy = __builtin_amdgcn_permlane32_swap(vo[1], vo[3], false, false);
                vo = sanit(u32x4{vx[0], vy[0], vx[1], vy[1]});
                const unsigned go = kvb + (unsigned)(h * 64 + 2 * tt + kh) * 2048 + lane * 16;
                ST128(ko, rs, go);
                ST128(vo, rs, go + 1024);
                char* lo = smem + A_KV + hw * 20480 + (8 + kh) * 2048 + lane * 16;
                *(u32x4*)(lo) = ko;
                *(u32x4*)(lo + 1024) = vo;
                if (w == 0) *(u32x4*)(smem + A_ONES + lane * 16) = u32x4{0x3f803f80u, 0x3f803f80u, 0x3f803f80u, 0x3f803f80u};
            }
            STAMP(16 * layer + 6);
            f32x4 aq[2][2];
#pragma unroll
            for (int i = 0; i < 2; ++i) { aq[i][0] = f32x4{0.f, 0.f, 0.f, 0.f}; aq[i][1] = f32x4{0.f, 0.f, 0.f, 0.f}; }
            bf16x8 xq[6][2];
#pragma unroll
            for (int i = 0; i < 6; ++i) { TRLD(xq[i], i); }
#define KSRC(i) (ksrc0 + (i) * 1024)
#define KDST(i) (kdst0 + (i) * 1024)
            const char* ksrc0 = ws + kvb + (size_t)(h * 64 + P0) * 2048 + lane * 16;
            char* kdst0 = smem + A_KV + hw * 20480 + 4 * kh * 2048;
#pragma unroll
            for (int iq = 0; iq < 16; ++iq) {
                SB();
                if (iq == QD) DMA_ISSUE(8, KSRC, KDST, live);
                const int i = iq;
                SB();
#pragma unroll
                for (int ti = 0; ti < 2; ++ti) aq[ti][i & 1] = MFMA16(Wq[i], xq[i % 6][ti], aq[ti][i & 1]);
                SB();
                if (i + 6 < 16) { TRLD(xq[i % 6], i + 6); }
            }
            {
                const float qs = 0.25f * 1.4426950408889634f;
                u32x4 qo;
#pragma unroll
                for (int i = 0; i < 2; ++i) {
                    f32x4 v = aq[i][0] + aq[i][1];
                    float qv[4];
#pragma unroll
                    for (int r = 0; r < 4; ++r) qv[r] = rbf((v[r] - m0[i] * csq[r]) * r0[i]) * qs;
                    qo[2 * i] = pk(qv[0], qv[1]); qo[2 * i + 1] = pk(qv[2], qv[3]);
                }
                auto qx = __builtin_amdgcn_permlane16_swap(qo[0], qo[2], false, false);
                auto qy = __builtin_amdgcn_permlane16_swap(qo[1], qo[3], false, false);
                Qf = __builtin_bit_cast(bf16x8, (u32x4{qx[0], qy[0], qx[1], qy[1]}));
            }
            STAMP(16 * layer + 10);
        }
        bf16x8 Wo[16];
        {
            PHASE_IDS();
            {
                const char* ksrc0 = ws + kvb + (size_t)(h * 64 + P0) * 2048 + lane * 16;
                char* kdst0 = smem + A_KV + hw * 20480 + 4 * kh * 2048;
                if (QD < 16) { DMA_VALID2(8, KSRC, KDST, live); } else { DMA_VALID(8, KSRC, KDST, live); }
            }
            BAR();
            STAMP(16 * layer + 12);
            if (layer == 0) {
                const u32x4 pz = {POISON, POISON, POISON, POISON};
                ST128(pz, rs, OFF_XT3 + (unsigned)(b * 512 + tid) * 16);
                ST128(pz, rs, OFF_KV + 4 * MB + (unsigned)(b * 1024 + tid) * 16);
                ST128(pz, rs, OFF_KV + 4 * MB + (unsigned)(b * 1024 + 512 + tid) * 16);
                ST128(pz, rs, OFF_OF + 2 * MB + (unsigned)(b * 512 + tid) * 16);
            }
            const bf16x8* wo = (const bf16x8*)(ws + OFF_WOF) + (size_t)((4 * hg + hw) * 16) * 64 + lane;
            const int ab = 2 * tt + kh;
            u32x4 o = ab < 8 ? attn_body<true>(smem, hw, kh, lane, ab, Qf, wo, Wo) : attn_fast(smem, hw, kh, lane, Qf, wo, Wo);
            {
                const int tile = 2 * ab + (g & 1);
                const int gp = 2 * (h & 1) + (g >> 1);
                ST128(sanit(o), rs, ofb + (unsigned)((tile * 16 + (h >> 1)) * 64 + l15 + 16 * gp) * 16);
                SB();
#pragma unroll
                for (int ks = 8; ks < 16; ++ks) Wo[ks] = wo[ks * 64];
            }
            STAMP(16 * layer + 9);
            if (!OEARLY) BAR();
            STAMP(16 * layer + 13);
        }
        {
            PHASE_IDS();
            STAMP(16 * layer + 14);
            {
                const char* src0 = ws + ofb + (size_t)(4 * tt * 16 + 32 * kh + hw) * 1024 + lane * 16;
#define OSRC(i) (src0 + (i) * 4096)
#define ODST(i) (smem + 1024 * (32 * kh + hw + 4 * (i)))
                DMA_VALID(8, OSRC, ODST, 0xFFu);
            }
            BAR();
            STAMP(16 * layer + 11);
            if (layer == 0) { RLOAD(); SB(); }
            f32x4 acc[2][2];
#pragma unroll
            for (int i = 0; i < 2; ++i) { acc[i][0] = f32x4{0.f, 0.f, 0.f, 0.f}; acc[i][1] = f32x4{0.f, 0.f, 0.f, 0.f}; }
            {
                const char* op = smem + (2 * kh) * 16384 + lane * 16;
                bf16x8 of[2][8], og[2][8];
#pragma unroll
                for (int ks = 0; ks < 8; ++ks)
#pragma unroll
                    for (int i = 0; i < 2; ++i) of[i][ks] = *(const bf16x8*)(op + (i * 16 + ks) * 1024);
                SB();
#pragma unroll
                for (int ks = 0; ks < 8; ++ks) {
#pragma unroll
                    for (int i = 0; i < 2; ++i) og[i][ks] = *(const bf16x8*)(op + (i * 16 + 8 + ks) * 1024);
#pragma unroll
                    for (int i = 0; i < 2; ++i) acc[i][ks & 1] = MFMA16(of[i][ks], Wo[ks], acc[i][ks & 1]);
                    SB();
                }
#pragma unroll
                for (int ks = 0; ks < 8; ++ks)
#pragma unroll
                    for (int i = 0; i < 2; ++i) acc[i][ks & 1] = MFMA16(og[i][ks], Wo[8 + ks], acc[i][ks & 1]);
            }
            u32x2 o[2];
#pragma unroll
            for (int i = 0; i < 2; ++i) {
                f32x4 v = acc[i][0] + acc[i][1];
                o[i][0] = pk(bflo(xres[i][0]) + rbf(v[0]), bfhi(xres[i][0]) + rbf(v[1]));
                o[i][1] = pk(bflo(xres[i][1]) + rbf(v[2]), bfhi(xres[i][1]) + rbf(v[3]));
            }
            if (layer == 0) {
                float sel = 0.f;
#pragma unroll
                for (int i = 0; i < 2; ++i)
#pragma unroll
                    for (int r = 0; r < 4; ++r) {
                        const float x = (r & 1) ? bfhi(o[i][r >> 1]) : bflo(o[i][r >> 1]);
                        const float a1 = row16_sum(x), a2 = row16_sum(x * x);
                        if (l15 == 2 * (4 * i + r)) sel = a1;
                        if (l15 == 2 * (4 * i + r) + 1) sel = a2;
                    }
                {
                    const int e = l15 >> 1;
                    const int t = 64 * tt + 32 * kh + 16 * (e >> 2) + 4 * g + (e & 3);
                    const float sc = (l15 & 1) ? 16777216.0f : 1073741824.0f;
                    long long iv = (long long)__builtin_rintf(fminf(fmaxf(sel * sc, -1.0e15f), 1.0e15f)) + (1ll << 51) + (1ll << 58);
                    atomicAdd((unsigned long long*)(ws + OFF_ACC) + (size_t)t * 2 + (l15 & 1), (unsigned long long)iv);
                }
                SB();
                {
                    auto sx = __builtin_amdgcn_permlane16_swap(o[0][0], o[1][0], false, false);
                    auto sy = __builtin_amdgcn_permlane16_swap(o[0][1], o[1][1], false, false);
                    u32x4 ov = {sx[0], sy[0], sx[1], sy[1]};
                    ST128(sanit(ov), rs, OFF_XT2 + (unsigned)(64 * hg + 16 * hw + l15) * (T * 2) + 128 * tt + 64 * kh + 32 * (g & 1) + 16 * (g >> 1));
                }
                BAR();
            } else {
                BAR();
                u16* img = (u16*)smem;
#pragma unroll
                for (int i = 0; i < 2; ++i)
#pragma unroll
                    for (int r = 0; r < 4; ++r)
                        img[(32 * kh + 16 * i + 4 * g + r) * 72 + 16 * hw + l15] = (u16)((r & 1) ? (o[i][r >> 1] >> 16) : (o[i][r >> 1] & 0xffff));
                BAR();
                const int t = tid >> 3, nc = tid & 7;
                u32x4 r = *(const u32x4*)(img + t * 72 + 8 * nc);
                const __amdgpu_buffer_rsrc_t ro = __builtin_amdgcn_make_buffer_rsrc((void*)a.out, 0, T * D * 2, 0x00020000);
                ST128(r, ro, (unsigned)((64 * tt + t) * D + 64 * hg + 8 * nc) * 2u);
            }
            STAMP(16 * layer + 15);
        }
    }
}

__global__ __launch_bounds__(512, 2) void k_mega(MegaArgs a) {
    extern __shared__ __attribute__((aligned(16))) char smem[];
    char* ws = a.ws;
    STAMP(0);
    u32x4 Rv[4];
    layer_body<0>(a, smem, Rv);
    layer_body<1>(a, smem, Rv);
}

extern "C" void kernel_launch(void* const* d_in, const int* in_sizes, int n_in, void* d_out, int out_size, void* d_ws,
                              size_t ws_size, hipStream_t stream) {
    char* ws = (char*)d_ws;
    PrepArgs pa;
    pa.x = (const u16*)d_in[0];
    pa.wqkv = (const u16*)d_in[10];
    pa.wo = (const u16*)d_in[11];
    pa.Are = (const u16*)d_in[1];
    pa.Aim = (const u16*)d_in[2];
    pa.Bre = (const u16*)d_in[3];
    pa.Bim = (const u16*)d_in[4];
    pa.Cre = (const u16*)d_in[5];
    pa.Cim = (const u16*)d_in[6];
    pa.logdt = (const u16*)d_in[7];
    pa.ws = ws;
    MegaArgs ma;
    ma.Dskip = (const u16*)d_in[8];
    ma.out = (u16*)d_out;
    ma.ws = ws;
    (void)hipFuncSetAttribute((const void*)k_mega, hipFuncAttributeMaxDynamicSharedMemorySize, MEGA_LDS);
    k_prep<<<256, 512, 0, stream>>>(pa);
    k_mega<<<256, 512, MEGA_LDS, stream>>>(ma);
}
```
